```python
import torch

def _softmax(x, axis=-1):
    return torch.softmax(x, dim=axis)
def _gelu(x):
    return torch.nn.functional.gelu(x, approximate="tanh")
def _silu(x):
    return torch.nn.functional.silu(x)
def _sigmoid(x):
    return torch.sigmoid(x)
def _layernorm(x, axis=-1, eps=1e-5):
    m = x.mean(dim=axis, keepdim=True)
    v = x.var(dim=axis, keepdim=True, unbiased=False)
    return (x - m) / torch.sqrt(v + eps)
def _scatter_add(tgt, idx, src, axis=0):
    return tgt.clone().index_add_(axis, idx, src)
def _segment_sum(src, seg, n):
    out = torch.zeros((n,) + src.shape[1:], dtype=src.dtype)
    return out.index_add_(0, seg, src)
def _pad(x, pw):
    flat = tuple(v for pair in reversed(pw) for v in pair)
    return torch.nn.functional.pad(x, flat)

kernel_name = 'sequential_preLN__grouped_kv_mqa__mixer_s_mlp__prompt'

def setup_inputs():

    _w = (lambda a: (lambda t: t.to(torch.bfloat16) if t.is_floating_point() else t)(torch.as_tensor(a)))
    def _A_setup():
        B, S, D, H = 1, 2048, 512, 32
        Dh = D // H
        return {
            "x": _w((lambda *s: torch.randn(*s, dtype=torch.bfloat16) * 0.05)(B, S, D)),
            "wq": _w((lambda *s: torch.randn(*s, dtype=torch.bfloat16) * 0.05)(D, H * Dh)),
            "wk": _w((lambda *s: torch.randn(*s, dtype=torch.bfloat16) * 0.05)(D, Dh)),
            "wv": _w((lambda *s: torch.randn(*s, dtype=torch.bfloat16) * 0.05)(D, Dh)),
            "wo": _w((lambda *s: torch.randn(*s, dtype=torch.bfloat16) * 0.05)(H * Dh, D)),
        }
    _A_in = _A_setup()

    def _B_setup():
        B, S, D, F = 1, 2048, 512, 1792
        return {
            "x":  _w((lambda *s: torch.randn(*s, dtype=torch.bfloat16) * 0.05)(B, S, D)),
            "w1": _w((lambda *s: torch.randn(*s, dtype=torch.bfloat16) * 3.2)(D, D) * (D ** -0.5)),
            "w2": _w((lambda *s: torch.randn(*s, dtype=torch.bfloat16) * 3.2)(D, D) * (D ** -0.5)),
            "w3": _w((lambda *s: torch.randn(*s, dtype=torch.bfloat16) * 3.2)(D, F) * (D ** -0.5)),
            "w4": _w((lambda *s: torch.randn(*s, dtype=torch.bfloat16) * 1.6)(F, D) * (F ** -0.5)),
        }
    _B_in = _B_setup()

    return {
        "A_x": _A_in["x"],
        "A_wq": _A_in["wq"],
        "A_wk": _A_in["wk"],
        "A_wv": _A_in["wv"],
        "A_wo": _A_in["wo"],
        "B_x": _B_in["x"],
        "B_w1": _B_in["w1"],
        "B_w2": _B_in["w2"],
        "B_w3": _B_in["w3"],
        "B_w4": _B_in["w4"],
    }

def reference(A_x, A_wq, A_wk, A_wv, A_wo, B_x, B_w1, B_w2, B_w3, B_w4):
    def _A_ref(x, wq, wk, wv, wo):
        B, S, D = x.shape
        H = 32
        Dh = D // H
        q = (lambda x, *d: x.permute(*d))((x @ wq).reshape(B, S, H, Dh), 0, 2, 1, 3)
        k = x @ wk
        v = x @ wv
        scores = torch.einsum("bhqd,bkd->bhqk", q, k) * (Dh ** -0.5)
        p = _softmax(scores, axis=-1)
        o = torch.einsum("bhqk,bkd->bhqd", p, v)
        return (lambda x, *d: x.permute(*d))(o, 0, 2, 1, 3).reshape(B, S, H * Dh) @ wo

    def _B_ref(x, w1, w2, w3, w4):
        B, S, D = x.shape
        h = _gelu(_layernorm(x, axis=-1) @ w1)
        c = D // 4
        g0 = _pad(h[:, :, 0*c:1*c], ((0, 0), (1, 0), (0, 0)))[:, :S, :]
        g1 = _pad(h[:, :, 1*c:2*c], ((0, 0), (0, 1), (0, 0)))[:, 1:, :]
        g2 = _pad(h[:, :, 2*c:3*c], ((0, 0), (2, 0), (0, 0)))[:, :S, :]
        g3 = _pad(h[:, :, 3*c:   ], ((0, 0), (0, 2), (0, 0)))[:, 2:, :]
        hs = torch.concatenate([g0, g1, g2, g3], axis=-1) @ w2
        x1 = x + hs
        h2 = _gelu(_layernorm(x1, axis=-1) @ w3) @ w4
        return x1 + h2

    x = A_x
    h = x + _A_ref(_layernorm(x, axis=-1), A_wq, A_wk, A_wv, A_wo)
    h2 = h + _B_ref(_layernorm(h, axis=-1), B_w1, B_w2, B_w3, B_w4)
    return h2
```

```cpp
#include <hip/hip_runtime.h>
#include <hip/hip_bf16.h>
#include <stdint.h>

typedef __hip_bfloat16 bf16;
#define S_ 2048
#define D_ 512
#define F_ 1792

__device__ __forceinline__ float bf2f(bf16 v) { return __bfloat162float(v); }
__device__ __forceinline__ bf16 f2bf(float v) { return __float2bfloat16(v); }
__device__ __forceinline__ float gelu_tanh(float x) {
    float u = 0.7978845608028654f * (x + 0.044715f * x * x * x);
    return 0.5f * x * (1.f + tanhf(u));
}

__global__ void ln_k(const bf16* in, bf16* out) {
    int row = blockIdx.x; int lane = threadIdx.x;
    float v[8]; float s = 0;
    for (int i = 0; i < 8; i++) { v[i] = bf2f(in[row * D_ + lane * 8 + i]); s += v[i]; }
    for (int o = 32; o; o >>= 1) s += __shfl_xor(s, o);
    float m = s / D_; float q = 0;
    for (int i = 0; i < 8; i++) { float d = v[i] - m; q += d * d; }
    for (int o = 32; o; o >>= 1) q += __shfl_xor(q, o);
    float r = rsqrtf(q / D_ + 1e-5f);
    for (int i = 0; i < 8; i++) out[row * D_ + lane * 8 + i] = f2bf((v[i] - m) * r);
}
__global__ void gemm_k(const bf16* A, const bf16* B, bf16* C, const bf16* R1, const bf16* R2, int M, int N, int K, int act) {
    int n = blockIdx.x * 64 + (threadIdx.x & 63); int m = blockIdx.y * 4 + (threadIdx.x >> 6);
    float acc = 0;
    for (int k = 0; k < K; k++) acc += bf2f(A[m * K + k]) * bf2f(B[k * N + n]);
    if (act) acc = gelu_tanh(acc);
    if (R1) acc += bf2f(R1[m * N + n]);
    if (R2) acc += bf2f(R2[m * N + n]);
    C[m * N + n] = f2bf(acc);
}
__global__ void attn_k(const bf16* q, const bf16* k, const bf16* v, bf16* o) {
    int t = blockIdx.x * 8 + (threadIdx.x >> 5); int h = threadIdx.x & 31;
    float qq[16]; for (int d = 0; d < 16; d++) qq[d] = bf2f(q[t * D_ + h * 16 + d]) * 0.25f;
    float m = -1e30f, l = 0; float acc[16]; for (int d = 0; d < 16; d++) acc[d] = 0;
    for (int j = 0; j < S_; j++) {
        float s = 0; for (int d = 0; d < 16; d++) s += qq[d] * bf2f(k[j * 16 + d]);
        float mn = fmaxf(m, s); float a = __expf(m - mn); float p = __expf(s - mn);
        l = l * a + p; for (int d = 0; d < 16; d++) acc[d] = acc[d] * a + p * bf2f(v[j * 16 + d]);
        m = mn;
    }
    for (int d = 0; d < 16; d++) o[t * D_ + h * 16 + d] = f2bf(acc[d] / l);
}
__global__ void shift_k(const bf16* h, bf16* o) {
    int idx = blockIdx.x * 256 + threadIdx.x; int t = idx / D_, c = idx % D_;
    int g = c >> 7; int sh = g == 0 ? -1 : g == 1 ? 1 : g == 2 ? -2 : 2; int ts = t + sh;
    o[idx] = (ts >= 0 && ts < S_) ? h[ts * D_ + c] : f2bf(0.f);
}
__global__ void add_k(const bf16* a, const bf16* b, bf16* o) { int idx = blockIdx.x * 256 + threadIdx.x; o[idx] = f2bf(bf2f(a[idx]) + bf2f(b[idx])); }

extern "C" void kernel_launch(void* const* d_in, const int* in_sizes, int n_in, void* d_out, int out_size, void* d_ws, size_t ws_size, hipStream_t stream) {
    const bf16* x = (const bf16*)d_in[0]; const bf16* wq = (const bf16*)d_in[1]; const bf16* wk = (const bf16*)d_in[2]; const bf16* wv = (const bf16*)d_in[3]; const bf16* wo = (const bf16*)d_in[4];
    const bf16* w1 = (const bf16*)d_in[6]; const bf16* w2 = (const bf16*)d_in[7]; const bf16* w3 = (const bf16*)d_in[8]; const bf16* w4 = (const bf16*)d_in[9];
    bf16* ws = (bf16*)d_ws; const size_t T = (size_t)S_ * D_;
    bf16 *xn = ws, *q = ws + T, *k = ws + 2 * T, *v = ws + 3 * T, *o = ws + 4 * T, *h = ws + 5 * T, *xb = ws + 6 * T, *xb2 = ws + 7 * T, *hh = ws + 8 * T, *hsft = ws + 9 * T, *x1 = ws + 10 * T, *x1n = ws + 11 * T, *g = ws + 12 * T, *y = ws + 20 * T;
    bf16* out = (bf16*)d_out;
    ln_k<<<S_, 64, 0, stream>>>(x, xn);
    gemm_k<<<dim3(D_ / 64, S_ / 4), 256, 0, stream>>>(xn, wq, q, nullptr, nullptr, S_, D_, D_, 0);
    extern __global__ void gemm16_k(const bf16*, const bf16*, bf16*);
    gemm16_k<<<S_ / 16, 256, 0, stream>>>(xn, wk, k);
    gemm16_k<<<S_ / 16, 256, 0, stream>>>(xn, wv, v);
    attn_k<<<S_ / 8, 256, 0, stream>>>(q, k, v, o);
    gemm_k<<<dim3(D_ / 64, S_ / 4), 256, 0, stream>>>(o, wo, h, x, nullptr, S_, D_, D_, 0);
    ln_k<<<S_, 64, 0, stream>>>(h, xb);
    ln_k<<<S_, 64, 0, stream>>>(xb, xb2);
    gemm_k<<<dim3(D_ / 64, S_ / 4), 256, 0, stream>>>(xb2, w1, hh, nullptr, nullptr, S_, D_, D_, 1);
    shift_k<<<S_ * D_ / 256, 256, 0, stream>>>(hh, hsft);
    gemm_k<<<dim3(D_ / 64, S_ / 4), 256, 0, stream>>>(hsft, w2, x1, xb, nullptr, S_, D_, D_, 0);
    ln_k<<<S_, 64, 0, stream>>>(x1, x1n);
    gemm_k<<<dim3(F_ / 64, S_ / 4), 256, 0, stream>>>(x1n, w3, g, nullptr, nullptr, S_, F_, D_, 1);
    gemm_k<<<dim3(D_ / 64, S_ / 4), 256, 0, stream>>>(g, w4, out, x1, h, S_, D_, F_, 0);
}
__global__ void gemm16_k(const bf16* A, const bf16* B, bf16* C) {
    int n = threadIdx.x & 15; int m = blockIdx.x * 16 + (threadIdx.x >> 4);
    float acc = 0;
    for (int k = 0; k < D_; k++) acc += bf2f(A[m * D_ + k]) * bf2f(B[k * 16 + n]);
    C[m * 16 + n] = f2bf(acc);
}
```

```cpp
#include <hip/hip_runtime.h>
#include <stdint.h>

typedef float f32x2 __attribute__((ext_vector_type(2)));
typedef float f32x4 __attribute__((ext_vector_type(4)));
typedef float f32x8 __attribute__((ext_vector_type(8)));
typedef float f32x16 __attribute__((ext_vector_type(16)));
typedef __bf16 bf16x2 __attribute__((ext_vector_type(2)));
typedef __bf16 bf16x8 __attribute__((ext_vector_type(8)));
typedef __bf16 bf16x16 __attribute__((ext_vector_type(16)));
typedef unsigned u32x2 __attribute__((ext_vector_type(2)));
typedef unsigned u32x4 __attribute__((ext_vector_type(4)));
typedef short s16x4 __attribute__((ext_vector_type(4)));
typedef unsigned long long u64;
#define LDSP __attribute__((address_space(3)))
#define GASP __attribute__((address_space(1)))
#define DEVI __device__ __forceinline__

constexpr unsigned OFF_E   = 0;
constexpr unsigned OFF_KVF = 4096;
constexpr unsigned OFF_FO  = 8192;
constexpr unsigned OFF_FH  = OFF_FO + 16384;
constexpr unsigned OFF_FHH = OFF_FH + 16384;
constexpr unsigned OFF_FX1 = OFF_FHH + 16384;
constexpr unsigned OFF_FG  = OFF_FX1 + 16384;
constexpr unsigned OFF_KI  = 0x40000;
constexpr unsigned OFF_VI  = OFF_KI + 0x10000;
constexpr unsigned OFF_HS  = 0x80000;
constexpr unsigned OFF_XS  = 0xC0000;
constexpr unsigned OFF_O   = 0x100000;
constexpr unsigned OFF_H   = 0x300000;
constexpr unsigned OFF_HH  = 0x500000;
constexpr unsigned OFF_X1  = 0x700000;
constexpr unsigned OFF_G   = 0x900000;
constexpr unsigned OFF_DBG = 0x1000000;
constexpr unsigned WS_USED = 0x1100000;

constexpr unsigned R0 = 0, R1 = 65536, R2 = 131072;
constexpr unsigned LDS_BYTES = 163840;

DEVI unsigned pk2(float a, float b) { f32x2 f = {a, b}; return __builtin_bit_cast(unsigned, __builtin_convertvector(f, bf16x2)); }
DEVI float bflo(unsigned u) { return __builtin_bit_cast(float, u << 16); }
DEVI float bfhi(unsigned u) { return __builtin_bit_cast(float, u & 0xffff0000u); }
DEVI float dot2(unsigned a, unsigned b, float c) { return __builtin_amdgcn_fdot2_f32_bf16(__builtin_bit_cast(bf16x2, a), __builtin_bit_cast(bf16x2, b), c, false); }
DEVI unsigned fbits(float f) { return __builtin_bit_cast(unsigned, f); }
DEVI float bitsf(unsigned u) { return __builtin_bit_cast(float, u); }
DEVI float x32sum(float v) { auto r = __builtin_amdgcn_permlane32_swap(fbits(v), fbits(v), false, false); return bitsf(r[0]) + bitsf(r[1]); }
DEVI float x16sum(float v) { auto r = __builtin_amdgcn_permlane16_swap(fbits(v), fbits(v), false, false); return bitsf(r[0]) + bitsf(r[1]); }
DEVI float x32max(float v) { auto r = __builtin_amdgcn_permlane32_swap(fbits(v), fbits(v), false, false); return fmaxf(bitsf(r[0]), bitsf(r[1])); }
DEVI float x16max(float v) { auto r = __builtin_amdgcn_permlane16_swap(fbits(v), fbits(v), false, false); return fmaxf(bitsf(r[0]), bitsf(r[1])); }
template <int C, int RM> DEVI float dppf(float v) { return __builtin_bit_cast(float, __builtin_amdgcn_update_dpp(0, __builtin_bit_cast(int, v), C, RM, 0xf, true)); }
DEVI float wave_sum(float v) {
    v += dppf<0x111, 0xf>(v); v += dppf<0x112, 0xf>(v); v += dppf<0x114, 0xf>(v); v += dppf<0x118, 0xf>(v);
    v += dppf<0x142, 0xa>(v); v += dppf<0x143, 0xc>(v);
    return __builtin_bit_cast(float, __builtin_amdgcn_readlane(__builtin_bit_cast(int, v), 63));
}
DEVI float wave_max_nn(float v) {
    v = fmaxf(v, dppf<0x111, 0xf>(v)); v = fmaxf(v, dppf<0x112, 0xf>(v)); v = fmaxf(v, dppf<0x114, 0xf>(v)); v = fmaxf(v, dppf<0x118, 0xf>(v));
    v = fmaxf(v, dppf<0x142, 0xa>(v)); v = fmaxf(v, dppf<0x143, 0xc>(v));
    return __builtin_bit_cast(float, __builtin_amdgcn_readlane(__builtin_bit_cast(int, v), 63));
}
DEVI void bar() { asm volatile("s_waitcnt lgkmcnt(0)\n\ts_barrier" ::: "memory"); }
template <int N> DEVI void waitvm() { asm volatile("s_waitcnt vmcnt(%0)" ::"n"(N) : "memory"); }
#define SBAR() __builtin_amdgcn_sched_barrier(0)

DEVI void dma16(const void* sbase, unsigned voff, unsigned lds) {
    unsigned keep;
    asm volatile("s_mov_b32 %0, m0\n\ts_mov_b32 m0, %3\n\ts_nop 2\n\tglobal_load_lds_dwordx4 %1, %2\n\ts_mov_b32 m0, %0"
                 : "=&s"(keep) : "v"(voff), "s"(sbase), "s"(lds) : "memory");
}
DEVI bf16x8 trfrag(unsigned a0, unsigned a1) {
    s16x4 lo = __builtin_amdgcn_ds_read_tr16_b64_v4i16((LDSP s16x4*)a0);
    s16x4 hi = __builtin_amdgcn_ds_read_tr16_b64_v4i16((LDSP s16x4*)a1);
    return __builtin_bit_cast(bf16x8, __builtin_shufflevector(lo, hi, 0, 1, 2, 3, 4, 5, 6, 7));
}
DEVI u32x4 ldsr128(unsigned a) { return *(LDSP u32x4*)a; }
DEVI void ldsw128(unsigned a, u32x4 v) { *(LDSP u32x4*)a = v; }
DEVI f32x4 ldsrf4(unsigned a) { return *(LDSP f32x4*)a; }
DEVI void ldswf4(unsigned a, f32x4 v) { *(LDSP f32x4*)a = v; }
typedef __amdgpu_buffer_rsrc_t rsrc_t;
DEVI rsrc_t mkrsrc(const void* p, unsigned bytes) { return __builtin_amdgcn_make_buffer_rsrc((void*)p, 0, bytes, 0x00020000); }
DEVI u32x4 ld_sc1(rsrc_t r, unsigned voff, unsigned soff) { return __builtin_amdgcn_raw_buffer_load_b128(r, voff, soff, 16); }
DEVI u32x2 ld_sc1_64(rsrc_t r, unsigned voff, unsigned soff) { return __builtin_amdgcn_raw_buffer_load_b64(r, voff, soff, 16); }
DEVI void st_sc1(u32x4 v, rsrc_t r, unsigned voff, unsigned soff) { __builtin_amdgcn_raw_buffer_store_b128(v, r, voff, soff, 16); }
DEVI void st_sc1_64(u32x2 v, rsrc_t r, unsigned voff, unsigned soff) { __builtin_amdgcn_raw_buffer_store_b64(v, r, voff, soff, 16); }
DEVI void flag_store(char* ws, unsigned off, u64 v) { __hip_atomic_store((GASP u64*)(ws + off), v, __ATOMIC_RELAXED, __HIP_MEMORY_SCOPE_AGENT); }
DEVI u64 flag_load(char* ws, unsigned off) { return __hip_atomic_load((GASP u64*)(ws + off), __ATOMIC_RELAXED, __HIP_MEMORY_SCOPE_AGENT); }
constexpr int SPIN_MAX = 40000;
DEVI void poll_flags(char* ws, unsigned off, bool need, u64 tag, int& dead) {
    if (dead) return;
    for (int it = 0; it < SPIN_MAX; it++) {
        u64 v = need ? flag_load(ws, off) : tag;
        if (__all(v == tag)) { asm volatile("" ::: "memory"); return; }
        __builtin_amdgcn_s_sleep(1);
        asm volatile("" ::: "memory");
    }
    dead = 1;
}
DEVI float gelu(float x) {
    float z = x * fmaf(x * x, -0.10294324f, -2.3022082f);
    float e = __builtin_amdgcn_exp2f(z);
    return x * __builtin_amdgcn_rcpf(1.f + e);
}
DEVI f32x4 mfma16(bf16x8 a, bf16x8 b, f32x4 c) { return __builtin_amdgcn_mfma_f32_16x16x32_bf16(a, b, c, 0, 0, 0); }
DEVI f32x16 mfma32(bf16x8 a, bf16x8 b, f32x16 c) { return __builtin_amdgcn_mfma_f32_32x32x16_bf16(a, b, c, 0, 0, 0); }
DEVI bf16x8 asbf(u32x4 v) { return __builtin_bit_cast(bf16x8, v); }
template <bool B> struct BoolC { static constexpr bool value = B; };
DEVI int swz128(int row) { return (((row >> 1) & 1) << 1) | ((row >> 2) & 1); }

#define STAMP(i) do {} while (0)
#define CSTAMP(i) do {} while (0)
#define STAMP_DECL do {} while (0)
#define STAMP_FLUSH() do {} while (0)

template <int NI>
DEVI void dma_w128(const char* src, unsigned stride, unsigned lds, int lane) {
    const int r = lane >> 3, c = lane & 7;
    const unsigned voff = r * stride + ((((c >> 1) ^ swz128(r)) << 1) | (c & 1)) * 16;
#pragma unroll
    for (int i = 0; i < NI; i++) dma16(src + (size_t)i * 8 * stride, voff, lds + i * 1024);
}

DEVI f32x16 pvmac(bf16x8 v, const f32x16& s, int half, f32x16 o) {
    f32x8 p = half == 0 ? __builtin_shufflevector(s, s, 0, 1, 2, 3, 4, 5, 6, 7) : __builtin_shufflevector(s, s, 8, 9, 10, 11, 12, 13, 14, 15);
    return __builtin_amdgcn_smfmac_f32_32x32x32_bf16(v, __builtin_bit_cast(bf16x16, p), o, 0xDDDDDDDD, 0, 0);
}
template <int A, int B> DEVI void exps(f32x16& s) {
#pragma unroll
    for (int i = A; i < B; i++) s[i] = __builtin_amdgcn_exp2f(s[i]);
}

DEVI unsigned opq(unsigned a) { asm volatile("" : "+v"(a)); return a; }
template <int NS, int D2, int ST, int NT, int DEPTH>
DEVI void gemm_p(const unsigned (&wa)[NT], const u32x4* bf, f32x4 (&acc)[NT]) {
    bf16x8 fr[DEPTH + 1][NT];
#pragma unroll
    for (int d = 0; d < DEPTH; d++)
#pragma unroll
        for (int nt = 0; nt < NT; nt++) fr[d][nt] = trfrag(wa[nt] + d * ST, wa[nt] + d * ST + D2);
#pragma unroll
    for (int s = 0; s < NS; s++) {
        SBAR();
        if (s + DEPTH < NS) {
#pragma unroll
            for (int nt = 0; nt < NT; nt++) fr[(s + DEPTH) % (DEPTH + 1)][nt] = trfrag(wa[nt] + (s + DEPTH) * ST, wa[nt] + (s + DEPTH) * ST + D2);
        }
        SBAR();
#pragma unroll
        for (int nt = 0; nt < NT; nt++) acc[nt] = mfma16(fr[s % (DEPTH + 1)][nt], asbf(bf[s]), acc[nt]);
    }
    SBAR();
}
template <int NS, int D2, int ST, int NT, int DEPTH>
DEVI void gemm_ones(const unsigned (&wa)[NT], f32x4 (&acc)[NT]) {
    const u32x4 ones = {0x3F803F80u, 0x3F803F80u, 0x3F803F80u, 0x3F803F80u};
    u32x4 of[NS];
#pragma unroll
    for (int s = 0; s < NS; s++) of[s] = ones;
    gemm_p<NS, D2, ST, NT, DEPTH>(wa, of, acc);
}
DEVI void lds_signal(unsigned addr, int lane) {
    asm volatile("s_waitcnt lgkmcnt(0)" ::: "memory");
    if (lane == 0) *(volatile LDSP unsigned*)addr = 1u;
}
DEVI void lds_wait4(unsigned addr, int& dead) {
    if (dead) return;
    for (int it = 0; it < SPIN_MAX; it++) {
        const u32x4 v = *(volatile LDSP u32x4*)addr;
        if (__builtin_amdgcn_readfirstlane(v[0] & v[1] & v[2] & v[3])) { asm volatile("" ::: "memory"); return; }
        __builtin_amdgcn_s_sleep(1);
    }
    dead = 1;
}

DEVI void combine_stats(rsrc_t rs, unsigned base, int tok, int g, float& mu, float& var) {
    u32x2 p[4];
#pragma unroll
    for (int i = 0; i < 4; i++) p[i] = ld_sc1_64(rs, ((4 * g + i) * 2048 + tok) * 8, base);
    float sm = bitsf(p[0][0]) + bitsf(p[1][0]) + bitsf(p[2][0]) + bitsf(p[3][0]);
    sm = x32sum(x16sum(sm));
    mu = sm * (1.f / 16.f);
    float m2 = 0.f, dv = 0.f;
#pragma unroll
    for (int i = 0; i < 4; i++) { m2 += bitsf(p[i][1]); float d = bitsf(p[i][0]) - mu; dv = fmaf(d, d, dv); }
    m2 = fmaf(dv, 32.f, m2);
    m2 = x32sum(x16sum(m2));
    var = m2 * (1.f / 512.f);
}
DEVI void piece_stats(const float (&v)[8], float& mean, float& m2) {
    float s = ((v[0] + v[1]) + (v[2] + v[3])) + ((v[4] + v[5]) + (v[6] + v[7]));
    s = x32sum(x16sum(s));
    mean = s * (1.f / 32.f);
    float q = 0.f;
#pragma unroll
    for (int i = 0; i < 8; i++) { float d = v[i] - mean; q = fmaf(d, d, q); }
    m2 = x32sum(x16sum(q));
}

__global__ void __launch_bounds__(512, 2)
fused_block_kernel(const char* __restrict__ x, const char* __restrict__ wq, const char* __restrict__ wk, const char* __restrict__ wv,
                   const char* __restrict__ wo, const char* __restrict__ w1, const char* __restrict__ w2, const char* __restrict__ w3,
                   const char* __restrict__ w4, char* __restrict__ out, char* __restrict__ ws) {
    extern __shared__ char smem[];
    const unsigned sm = (unsigned)(uintptr_t)(LDSP char*)smem;
    const int b = blockIdx.x, tid = threadIdx.x, lane = tid & 63;
    const int w = __builtin_amdgcn_readfirstlane(tid >> 6);
    const int grp = (b & 7) * 4 + (b >> 6), j = (b >> 3) & 7;
    const int T0 = grp * 64;
    int dead = 0;
    STAMP_DECL;
    STAMP(0); CSTAMP(62);
    const u64 epoch = flag_load(ws, OFF_E) + 1;
    const rsrc_t rws = mkrsrc(ws, WS_USED);

    constexpr unsigned XT = R0, WQ = R1, WK = R2, WV = R2 + 16384;
    constexpr unsigned LF = R2 + 32768 - 64;
    {
        const int rp = lane >> 1, r = rp ^ (((rp >> 3) & 1) << 2);
        const unsigned voff = r * 32 + (lane & 1) * 16;
#pragma unroll
        for (int i = 0; i < 2; i++) {
            dma16(wk + (2 * w + i) * 1024, voff, sm + WK + (2 * w + i) * 1024);
            dma16(wv + (2 * w + i) * 1024, voff, sm + WV + (2 * w + i) * 1024);
        }
    }
    const int rho = 8 * j + w;
    u32x4 xr;
    asm volatile("global_load_dwordx4 %0, %1, %2" : "=v"(xr) : "v"(lane * 16), "s"(x + (size_t)(T0 + rho) * 1024) : "memory");
    const int tq = w & 3, kh = w >> 2, g4 = lane >> 4, t16 = lane & 15;
    const int tokq = T0 + 16 * tq + t16;
    auto xrow_dma = [&](int i) {
        int r = w - 2 + 6 * i; r = r > 55 ? 55 : r;
        const int row = r < 8 * j ? r : r + 8;
        dma16(x + (size_t)(T0 + row) * 1024, (lane ^ (row & 15)) * 16, sm + XT + row * 1024);
    };
    constexpr int NB1 = 4;
    if (w >= 2) {
#pragma unroll
        for (int i = 0; i < NB1; i++) xrow_dma(i);
    }
    STAMP(17);
    asm volatile("s_cmp_lt_u32 %1, 2\n\ts_cbranch_scc1 1f\n\ts_waitcnt vmcnt(%2)\n\ts_branch 2f\n1:\n\ts_waitcnt vmcnt(0)\n2:" : "+v"(xr) : "s"(w), "n"(NB1) : "memory", "scc");
    STAMP(18);
    {
        float v[8];
#pragma unroll
        for (int i = 0; i < 4; i++) { v[2 * i] = bflo(xr[i]); v[2 * i + 1] = bfhi(xr[i]); }
        float s1 = 0.f, s2 = 0.f;
#pragma unroll
        for (int i = 0; i < 8; i++) { s1 += v[i]; s2 = fmaf(v[i], v[i], s2); }
        s1 = wave_sum(s1); s2 = wave_sum(s2);
        const float mu = s1 * (1.f / 512.f);
        const float var = fmaxf(s2 * (1.f / 512.f) - mu * mu, 0.f);
        const float r = __builtin_amdgcn_rsqf(var + 1e-5f);
        u32x4 o;
#pragma unroll
        for (int i = 0; i < 4; i++) o[i] = pk2((v[2 * i] - mu) * r, (v[2 * i + 1] - mu) * r);
        ldsw128(sm + XT + rho * 1024 + ((lane ^ (rho & 15)) * 16), o);
    }
    bar();
    STAMP(1);
    if (w < 2) {
        const int xrow = (8 * j + t16) & 63;
        const unsigned xa = sm + XT + xrow * 1024;
        const int q = t16 >> 2, p = t16 & 3;
        const int r0 = 8 * g4 + q, r1 = r0 + 4;
        const unsigned wbase = sm + (w == 0 ? WK : WV);
        const unsigned a0 = opq(wbase + (r0 ^ (((r0 >> 3) & 1) << 2)) * 32 + p * 8);
        const unsigned a1 = opq(wbase + (r1 ^ (((r1 >> 3) & 1) << 2)) * 32 + p * 8);
        unsigned xs[4];
#pragma unroll
        for (int i = 0; i < 4; i++) xs[i] = opq(xa + (((4 * i + g4) ^ (xrow & 15)) * 16));
        f32x4 acc0 = {}, acc1 = {};
        auto proj = [&](auto wfirst) {
            constexpr int DP = 4;
            u32x4 xf[DP + 1]; bf16x8 wf[DP + 1];
#pragma unroll
            for (int d = 0; d < DP; d++) { xf[d] = ldsr128(xs[d & 3] + (d >> 2) * 256); wf[d] = trfrag(a0 + d * 1024, a1 + d * 1024); }
#pragma unroll
            for (int s = 0; s < 16; s++) {
                SBAR();
                if (s + DP < 16) { const int n = s + DP; xf[n % (DP + 1)] = ldsr128(xs[n & 3] + (n >> 2) * 256); wf[n % (DP + 1)] = trfrag(a0 + n * 1024, a1 + n * 1024); }
                SBAR();
                f32x4& acc = (s & 1) ? acc1 : acc0;
                if (wfirst.value) acc = mfma16(wf[s % (DP + 1)], asbf(xf[s % (DP + 1)]), acc);
                else acc = mfma16(asbf(xf[s % (DP + 1)]), wf[s % (DP + 1)], acc);
            }
            SBAR();
        };
        if (w == 0) proj(BoolC<true>{}); else proj(BoolC<false>{});
        const f32x4 acc = acc0 + acc1;
        if (w == 0) {
            const unsigned k01 = pk2(acc[0], acc[1]), k23 = pk2(acc[2], acc[3]);
            auto s0 = __builtin_amdgcn_permlane32_swap(k01, k01, false, false);
            auto s1 = __builtin_amdgcn_permlane32_swap(k23, k23, false, false);
            u32x4 kv = {s0[0], s1[0], s0[1], s1[1]};
            const int tile = 2 * grp + (j >> 2), kk = 8 * (j & 3) + t16;
            if (lane < 32 && t16 < 8) st_sc1(kv, rws, tile * 1024 + g4 * 512 + kk * 16, OFF_KI);
            float n2 = dot2(k01, k01, dot2(k23, k23, 0.f));
            n2 = x32sum(x16sum(n2));
            n2 = t16 < 8 ? n2 : 0.f;
            const float kmax2 = wave_max_nn(n2);
            const unsigned pay = (fbits(kmax2) + 0xFFFFu) >> 16;
            waitvm<0>();
            if (lane == 0) flag_store(ws, OFF_KVF + (grp * 8 + j) * 16, (epoch << 16) | (u64)(pay & 0xFFFFu));
        } else {
            const unsigned v01 = pk2(acc[0], acc[1]), v23 = pk2(acc[2], acc[3]);
            auto s0 = __builtin_amdgcn_permlane16_swap(v01, v01, false, false);
            auto s1 = __builtin_amdgcn_permlane16_swap(v23, v23, false, false);
            u32x4 vv = {s0[0], s1[0], s0[1], s1[1]};
            if (lane == 16) vv = u32x4{0x3F803F80u, 0x3F803F80u, 0x3F803F80u, 0x3F803F80u};
            const int tile = 2 * grp + (j >> 2), blk = j & 3;
            if (lane < 17) st_sc1(vv, rws, tile * 1536 + blk * 384 + lane * 16, OFF_VI);
            float am = fmaxf(fmaxf(fabsf(bflo(v01)), fabsf(bfhi(v01))), fmaxf(fabsf(bflo(v23)), fabsf(bfhi(v23))));
            am = lane < 32 ? am : 0.f;
            const float vmax = wave_max_nn(am);
            const unsigned pay = (fbits(vmax) + 0xFFFFu) >> 16;
            waitvm<0>();
            if (lane == 0) flag_store(ws, OFF_KVF + (grp * 8 + j) * 16 + 8, (epoch << 16) | (u64)(pay & 0xFFFFu));
        }
    } else {
#pragma unroll
        for (int i = NB1; i < 10; i++) xrow_dma(i);
        const int rr = lane >> 3, c = lane & 7;
        const unsigned voff = rr * 1024 + ((((c >> 1) ^ swz128(rr)) << 1) | (c & 1)) * 16;
#pragma unroll
        for (int i = 0; i < 11; i++) {
            int ii = w - 2 + 6 * i; ii = ii > 63 ? 63 : ii;
            dma16(wq + (size_t)ii * 8192 + j * 128, voff, sm + WQ + ii * 1024);
        }
    }
    waitvm<0>();
    bar();
    STAMP(2);
    bf16x8 qf[4];
    {
        if (tid < 16) *(LDSP unsigned*)(sm + LF + tid * 4) = 0u;
        const int nh = w & 1, th = (w >> 1) & 1, k2 = w >> 2;
        const int t32 = lane & 31, hi = lane >> 5;
        const int xrow = 32 * th + t32;
        const unsigned xa = sm + XT + xrow * 1024;
        const int q = t16 >> 2, p = t16 & 3, cb = 2 * nh + ((lane >> 4) & 1);
        const int r0 = 8 * hi + q;
        const int rot = th ^ nh;
        unsigned a0[2], a1[2], xs[2][8];
#pragma unroll
        for (int e = 0; e < 2; e++) {
            a0[e] = opq(sm + WQ + (256 * k2 + 128 * (e ^ rot) + r0) * 128 + ((cb ^ swz128(r0)) * 32) + p * 8);
            a1[e] = opq(sm + WQ + (256 * k2 + 128 * (e ^ rot) + r0 + 4) * 128 + ((cb ^ swz128(r0 + 4)) * 32) + p * 8);
#pragma unroll
            for (int i = 0; i < 8; i++) xs[e][i] = opq(xa + k2 * 512 + (e ^ rot) * 256 + (((2 * i + hi) ^ (xrow & 15)) * 16));
        }
        f32x16 acc = {}, cs = {};
        float s1 = 0.f, s2 = 0.f;
        const u32x4 ones = {0x3F803F80u, 0x3F803F80u, 0x3F803F80u, 0x3F803F80u};
        constexpr int DP = 2;
        u32x4 xf[DP + 1]; bf16x8 wf[DP + 1];
#pragma unroll
        for (int d = 0; d < DP; d++) { xf[d] = ldsr128(xs[d & 1][d >> 1]); wf[d] = trfrag(a0[d & 1] + (d >> 1) * 2048, a1[d & 1] + (d >> 1) * 2048); }
#pragma unroll
        for (int s = 0; s < 16; s++) {
            SBAR();
            if (s + DP < 16) { const int n = s + DP; xf[n % (DP + 1)] = ldsr128(xs[n & 1][n >> 1]); wf[n % (DP + 1)] = trfrag(a0[n & 1] + (n >> 1) * 2048, a1[n & 1] + (n >> 1) * 2048); }
            SBAR();
            acc = mfma32(wf[s % (DP + 1)], asbf(xf[s % (DP + 1)]), acc);
            if ((s & 1) == 0) {
                cs = mfma32(wf[s % (DP + 1)], asbf(ones), cs);
                const u32x4 xv = xf[s % (DP + 1)];
#pragma unroll
                for (int i = 0; i < 4; i++) { s1 = dot2(xv[i], 0x3F803F80u, s1); s2 = dot2(xv[i], xv[i], s2); }
            }
        }
        SBAR();
        STAMP(19);
        s1 = x32sum(s1); s2 = x32sum(s2);
        constexpr unsigned SC = R2;
        if (lane < 32) *(LDSP f32x2*)(sm + SC + ((th * 4 + nh * 2 + k2) * 32 + t32) * 8) = f32x2{s1, s2};
        if (t32 == 0) {
            const unsigned ca = sm + SC + 2048 + ((nh * 4 + th * 2 + k2) * 2 + hi) * 64;
#pragma unroll
            for (int i = 0; i < 4; i++) ldswf4(ca + i * 16, f32x4{cs[4 * i], cs[4 * i + 1], cs[4 * i + 2], cs[4 * i + 3]});
        }
        const unsigned xb = sm + SC + 3072;
        f32x4 g0, g1, k0, k1;
        if (k2 == 0) { g0 = f32x4{acc[8], acc[9], acc[10], acc[11]}; g1 = f32x4{acc[12], acc[13], acc[14], acc[15]}; k0 = f32x4{acc[0], acc[1], acc[2], acc[3]}; k1 = f32x4{acc[4], acc[5], acc[6], acc[7]}; }
        else { g0 = f32x4{acc[0], acc[1], acc[2], acc[3]}; g1 = f32x4{acc[4], acc[5], acc[6], acc[7]}; k0 = f32x4{acc[8], acc[9], acc[10], acc[11]}; k1 = f32x4{acc[12], acc[13], acc[14], acc[15]}; }
        ldswf4(xb + w * 2048 + lane * 16, g0);
        ldswf4(xb + w * 2048 + 1024 + lane * 16, g1);
        bar();
        STAMP(20);
        k0 += ldsrf4(xb + (w ^ 4) * 2048 + lane * 16);
        k1 += ldsrf4(xb + (w ^ 4) * 2048 + 1024 + lane * 16);
        f32x2 ss = {0.f, 0.f};
        f32x4 c0 = {}, c1 = {};
#pragma unroll
        for (int i = 0; i < 4; i++) {
            ss += *(LDSP f32x2*)(sm + SC + ((th * 4 + i) * 32 + t32) * 8);
            const unsigned ca = sm + SC + 2048 + ((nh * 4 + i) * 2 + hi) * 64 + k2 * 32;
            c0 += ldsrf4(ca); c1 += ldsrf4(ca + 16);
        }
        s1 = ss[0]; s2 = ss[1];
        float mu = s1 * (1.f / 512.f);
        const float var = fmaxf(s2 * (1.f / 512.f) - mu * mu, 0.f);
        float r = __builtin_amdgcn_rsqf(var + 1e-5f);
        if ((xrow >> 3) == j) { mu = 0.f; r = 1.f; }
        const float sc = r * (0.25f * 1.4426950408889634f);
        k0 = (k0 - mu * c0) * sc; k1 = (k1 - mu * c1) * sc;
        u32x4 qq = {pk2(k0[0], k0[1]), pk2(k0[2], k0[3]), pk2(k1[0], k1[1]), pk2(k1[2], k1[3])};
        const unsigned qb = sm + SC + 3072 + 16384;
        ldsw128(qb + (th * 4 + 2 * nh + k2) * 1024 + lane * 16, qq);
        bar();
        STAMP(21);
        const int th2 = w >> 2;
#pragma unroll
        for (int i = 0; i < 4; i++) qf[i] = asbf(ldsr128(qb + (th2 * 4 + i) * 1024 + lane * 16));
        bar();
    }
    STAMP(3);
    f32x16 oacc[4];
    float mrow[4];
    {
        const int kq = w & 3;
        float kmax2 = 0.f, vmax = 0.f;
        {
            const unsigned fo = OFF_KVF + (64 * kq + lane) * 16;
            const u64 tag = epoch & 0xFFFFFFFFFFFFull;
            if (!dead) {
                int it = 0;
                for (; it < SPIN_MAX; it++) {
                    u64 a = flag_load(ws, fo), c = flag_load(ws, fo + 8);
                    bool ok = ((a >> 16) == tag) && ((c >> 16) == tag);
                    kmax2 = bitsf((unsigned)(a & 0xFFFF) << 16); vmax = bitsf((unsigned)(c & 0xFFFF) << 16);
                    if (__all(ok)) break;
                    __builtin_amdgcn_s_sleep(1);
                    asm volatile("" ::: "memory");
                }
                asm volatile("" ::: "memory");
                if (it == SPIN_MAX) dead = 1;
            }
        }
        STAMP(4); CSTAMP(56);
        const unsigned koff = OFF_KI + kq * 16 * 1024, voffb = OFF_VI + kq * 16 * 1536;
        const int vr = lane & 31;
        const unsigned kl = lane * 16, vl = vr < 16 ? (lane >> 5) * 384 + vr * 16 : ((vr & 11) == 0 ? (lane >> 5) * 384 + 256 : 0x80000000u);
        u32x4 kf[3], v0[3], v1[3];
#pragma unroll
        for (int t = 0; t < 2; t++) {
            kf[t] = ld_sc1(rws, kl, koff + t * 1024);
            v0[t] = ld_sc1(rws, vl, voffb + t * 1536);
            v1[t] = ld_sc1(rws, vl, voffb + t * 1536 + 768);
        }
        kmax2 = wave_max_nn(kmax2); vmax = wave_max_nn(vmax);
        float qn = 0.f;
#pragma unroll
        for (int i = 0; i < 4; i++) {
            u32x4 qv = __builtin_bit_cast(u32x4, qf[i]);
            float t = dot2(qv[0], qv[0], dot2(qv[1], qv[1], dot2(qv[2], qv[2], dot2(qv[3], qv[3], 0.f))));
            qn = fmaxf(qn, x32sum(t));
        }
        qn = wave_max_nn(qn);
        const bool fast = (qn * kmax2 <= 3600.f) && (vmax <= 1.0e12f);
#pragma unroll
        for (int i = 0; i < 4; i++) { oacc[i] = f32x16{}; mrow[i] = 0.f; }
        const f32x16 zero = {};
        if (fast) {
            f32x16 sa = mfma32(asbf(kf[0]), qf[0], zero), sb = zero;
            constexpr int P0 = 1, P1 = 5, P2 = 10;
#pragma unroll
            for (int t = 0; t < 16; t++) {
                const int c = t % 3, n = (t + 1) % 3, nn = (t + 2) % 3, pv = (t + 2) % 3;
                SBAR();
                exps<0, P0>(sa); SBAR();
                if (t > 0) { oacc[3] = pvmac(asbf(v1[pv]), sb, 1, oacc[3]); SBAR(); }
                if (t + 2 < 16) {
                    kf[nn] = ld_sc1(rws, kl, koff + (t + 2) * 1024);
                    v0[nn] = ld_sc1(rws, vl, voffb + (t + 2) * 1536);
                    v1[nn] = ld_sc1(rws, vl, voffb + (t + 2) * 1536 + 768);
                    SBAR();
                }
                exps<P0, P1>(sa); SBAR();
                sb = mfma32(asbf(kf[c]), qf[1], zero); SBAR();
                exps<P1, P2>(sa); SBAR();
                oacc[0] = pvmac(asbf(v0[c]), sa, 0, oacc[0]); SBAR();
                exps<P2, 16>(sa); SBAR();
                exps<0, P0>(sb); SBAR();
                oacc[0] = pvmac(asbf(v1[c]), sa, 1, oacc[0]); SBAR();
                exps<P0, P1>(sb); SBAR();
                sa = mfma32(asbf(kf[c]), qf[2], zero); SBAR();
                exps<P1, P2>(sb); SBAR();
                oacc[1] = pvmac(asbf(v0[c]), sb, 0, oacc[1]); SBAR();
                exps<P2, 16>(sb); SBAR();
                exps<0, P0>(sa); SBAR();
                oacc[1] = pvmac(asbf(v1[c]), sb, 1, oacc[1]); SBAR();
                exps<P0, P1>(sa); SBAR();
                sb = mfma32(asbf(kf[c]), qf[3], zero); SBAR();
                exps<P1, P2>(sa); SBAR();
                oacc[2] = pvmac(asbf(v0[c]), sa, 0, oacc[2]); SBAR();
                exps<P2, 16>(sa); SBAR();
                exps<0, P0>(sb); SBAR();
                oacc[2] = pvmac(asbf(v1[c]), sa, 1, oacc[2]); SBAR();
                exps<P0, P1>(sb); SBAR();
                if (t + 1 < 16) { sa = mfma32(asbf(kf[n]), qf[0], zero); SBAR(); }
                exps<P1, P2>(sb); SBAR();
                oacc[3] = pvmac(asbf(v0[c]), sb, 0, oacc[3]); SBAR();
                exps<P2, 16>(sb); SBAR();
            }
            oacc[3] = pvmac(asbf(v1[15 % 3]), sb, 1, oacc[3]);
        } else {
#pragma unroll
            for (int i = 0; i < 4; i++) mrow[i] = -3.0e38f;
            for (int t = 0; t < 16; t++) {
                u32x4 kx = ld_sc1(rws, kl, koff + t * 1024);
                u32x4 va = ld_sc1(rws, vl, voffb + t * 1536);
                u32x4 vb = ld_sc1(rws, vl, voffb + t * 1536 + 768);
#pragma unroll
                for (int i = 0; i < 4; i++) {
                    f32x16 s = mfma32(asbf(kx), qf[i], zero);
                    float mx = s[0];
#pragma unroll
                    for (int e = 1; e < 16; e++) mx = fmaxf(mx, s[e]);
                    mx = x32max(mx);
                    const float mn = fmaxf(mrow[i], mx);
                    const float al = __builtin_amdgcn_exp2f(mrow[i] - mn);
                    mrow[i] = mn;
#pragma unroll
                    for (int e = 0; e < 16; e++) { s[e] = __builtin_amdgcn_exp2f(s[e] - mn); oacc[i][e] *= al; }
                    oacc[i] = pvmac(asbf(va), s, 0, oacc[i]);
                    oacc[i] = pvmac(asbf(vb), s, 1, oacc[i]);
                }
            }
        }
    }
    STAMP(5); CSTAMP(57);
    {
        const int th = w >> 2, kq = w & 3;
        constexpr unsigned RS = R2;
        if (th == 1) lds_wait4(sm + LF + 32, dead);
#pragma unroll
        for (int hd = 0; hd < 4; hd++) {
            if (hd != kq) {
                const unsigned a = sm + RS + (hd * 3 + (((kq - hd) & 3) - 1)) * 2560;
                ldswf4(a + lane * 16, f32x4{oacc[hd][0], oacc[hd][1], oacc[hd][2], oacc[hd][3]});
                ldswf4(a + 1024 + lane * 16, f32x4{oacc[hd][4], oacc[hd][5], oacc[hd][6], oacc[hd][7]});
                *(LDSP f32x2*)(a + 2048 + lane * 8) = f32x2{oacc[hd][8], mrow[hd]};
            }
        }
        lds_signal(sm + LF + th * 16 + kq * 4, lane);
        f32x4 oa, ob; float l, m;
        if (kq == 0) { oa = f32x4{oacc[0][0], oacc[0][1], oacc[0][2], oacc[0][3]}; ob = f32x4{oacc[0][4], oacc[0][5], oacc[0][6], oacc[0][7]}; l = oacc[0][8]; m = mrow[0]; }
        else if (kq == 1) { oa = f32x4{oacc[1][0], oacc[1][1], oacc[1][2], oacc[1][3]}; ob = f32x4{oacc[1][4], oacc[1][5], oacc[1][6], oacc[1][7]}; l = oacc[1][8]; m = mrow[1]; }
        else if (kq == 2) { oa = f32x4{oacc[2][0], oacc[2][1], oacc[2][2], oacc[2][3]}; ob = f32x4{oacc[2][4], oacc[2][5], oacc[2][6], oacc[2][7]}; l = oacc[2][8]; m = mrow[2]; }
        else { oa = f32x4{oacc[3][0], oacc[3][1], oacc[3][2], oacc[3][3]}; ob = f32x4{oacc[3][4], oacc[3][5], oacc[3][6], oacc[3][7]}; l = oacc[3][8]; m = mrow[3]; }
        lds_wait4(sm + LF + th * 16, dead);
        STAMP(22);
        f32x4 pa[3], pb[3]; f32x2 pl[3];
#pragma unroll
        for (int s = 0; s < 3; s++) {
            const unsigned a = sm + RS + (kq * 3 + s) * 2560;
            pa[s] = ldsrf4(a + lane * 16); pb[s] = ldsrf4(a + 1024 + lane * 16); pl[s] = *(LDSP f32x2*)(a + 2048 + lane * 8);
        }
        if (th == 0) lds_signal(sm + LF + 32 + kq * 4, lane);
        const float M = fmaxf(fmaxf(m, pl[0][1]), fmaxf(pl[1][1], pl[2][1]));
        const float f0 = __builtin_amdgcn_exp2f(m - M);
        oa *= f0; ob *= f0; l *= f0;
#pragma unroll
        for (int s = 0; s < 3; s++) {
            const float f = __builtin_amdgcn_exp2f(pl[s][1] - M);
            oa += pa[s] * f; ob += pb[s] * f; l = fmaf(pl[s][0], f, l);
        }
        const float il = 1.f / l;
        u32x4 ov = {pk2(oa[0] * il, oa[1] * il), pk2(oa[2] * il, oa[3] * il), pk2(ob[0] * il, ob[1] * il), pk2(ob[2] * il, ob[3] * il)};
        const int t32 = lane & 31, hi = lane >> 5;
        const int kstep = 2 * j + (kq >> 1), gp = 2 * (kq & 1) + hi;
        st_sc1(ov, rws, ((kstep * 4 + gp) * 2048 + T0 + 32 * th + t32) * 16, OFF_O);
        STAMP(23);
        if (th == 0) {
            waitvm<0>();
            if (lane == 0) flag_store(ws, OFF_FO + ((grp * 2 + th) * 32 + j * 4 + kq) * 8, epoch);
            dma_w128<16>(wo + (size_t)(128 * kq) * 1024 + j * 128, 1024, sm + R0 + kq * 16384, lane);
            dma_w128<16>(w1 + (size_t)(128 * kq) * 1024 + j * 128, 1024, sm + R1 + kq * 16384, lane);
            waitvm<0>();
        } else {
            waitvm<0>();
            if (lane == 0) flag_store(ws, OFF_FO + ((grp * 2 + th) * 32 + j * 4 + kq) * 8, epoch);
        }
        STAMP(24);
        bar();
        if (b == 0 && tid == 0) flag_store(ws, OFF_E, epoch);
    }
    STAMP(6); CSTAMP(59);
    const int Tq = grp * 4 + tq;
    const int lq = t16 >> 2, lp = t16 & 3;
    constexpr unsigned XS = R2;
    float hres[8], xres1[8];
    {
        u32x2 xres[2];
#pragma unroll
        for (int i = 0; i < 2; i++) xres[i] = *(const GASP u32x2*)(x + (size_t)tokq * 1024 + (64 * j + 32 * kh + 16 * i + 4 * g4) * 2);
        poll_flags(ws, OFF_FO + ((grp * 2 + (tq >> 1)) * 32 + 16 * kh + (lane & 15)) * 8, lane < 16, epoch, dead);
        STAMP(7);
        u32x4 bf[8];
#pragma unroll
        for (int s = 0; s < 8; s++) bf[s] = ld_sc1(rws, (g4 * 2048 + tokq) * 16, OFF_O + (8 * kh + s) * 4 * 2048 * 16);
        const int r0 = 16 * (g4 >> 1) + 4 * (g4 & 1) + lq;
        unsigned wa[4];
#pragma unroll
        for (int nt = 0; nt < 4; nt++) wa[nt] = opq(sm + R0 + (256 * kh + r0) * 128 + ((nt ^ swz128(r0)) * 32) + lp * 8);
        f32x4 acc[4] = {};
        gemm_p<8, 8 * 128, 4096, 4, 1>(wa, bf, acc);
        STAMP(26);
        ldswf4(sm + XS + w * 2048 + lane * 16, kh ? acc[0] : acc[2]);
        ldswf4(sm + XS + w * 2048 + 1024 + lane * 16, kh ? acc[1] : acc[3]);
        bar();
        f32x4 v0 = kh ? acc[2] : acc[0], v1 = kh ? acc[3] : acc[1];
        v0 += ldsrf4(sm + XS + (w ^ 4) * 2048 + lane * 16);
        v1 += ldsrf4(sm + XS + (w ^ 4) * 2048 + 1024 + lane * 16);
        STAMP(27);
        hres[0] = v0[0] + bflo(xres[0][0]); hres[1] = v0[1] + bfhi(xres[0][0]); hres[2] = v0[2] + bflo(xres[0][1]); hres[3] = v0[3] + bfhi(xres[0][1]);
        hres[4] = v1[0] + bflo(xres[1][0]); hres[5] = v1[1] + bfhi(xres[1][0]); hres[6] = v1[2] + bflo(xres[1][1]); hres[7] = v1[3] + bfhi(xres[1][1]);
        float mean, m2; piece_stats(hres, mean, m2);
        u32x4 hv = {pk2(hres[0], hres[1]), pk2(hres[2], hres[3]), pk2(hres[4], hres[5]), pk2(hres[6], hres[7])};
        st_sc1(hv, rws, (((2 * j + kh) * 4 + g4) * 2048 + tokq) * 16, OFF_H);
        if (g4 == 0) st_sc1_64(u32x2{fbits(mean), fbits(m2)}, rws, ((2 * j + kh) * 2048 + tokq) * 8, OFF_HS);
        dma_w128<8>(w2 + (size_t)(64 * w) * 1024 + j * 128, 1024, sm + R0 + w * 8192, lane);
        STAMP(28);
        waitvm<8>();
        if (lane == 0) flag_store(ws, OFF_FH + (Tq * 16 + 2 * j + kh) * 8, epoch);
    }
    STAMP(8);
    {
        const int r0 = 4 * g4 + lq;
        unsigned wa[4];
#pragma unroll
        for (int nt = 0; nt < 4; nt++) wa[nt] = opq(sm + R1 + (256 * kh + r0) * 128 + ((nt ^ swz128(r0)) * 32) + lp * 8);
        f32x4 cs[4] = {};
        gemm_ones<8, 16 * 128, 4096, 4, 1>(wa, cs);
        STAMP(29);
        poll_flags(ws, OFF_FH + (Tq * 16 + (lane & 15)) * 8, lane < 16, epoch, dead);
        STAMP(9);
        u32x4 bf[8];
#pragma unroll
        for (int s = 0; s < 8; s++) bf[s] = ld_sc1(rws, (g4 * 2048 + tokq) * 16, OFF_H + (8 * kh + s) * 4 * 2048 * 16);
        float mu, var; combine_stats(rws, OFF_HS, tokq, g4, mu, var);
        f32x4 acc[4] = {};
        gemm_p<8, 16 * 128, 4096, 4, 1>(wa, bf, acc);
        STAMP(30);
        const float r = __builtin_amdgcn_rsqf(var + 1e-5f);
        const float r2 = r * __builtin_amdgcn_rsqf(var * r * r + 1e-5f);
#pragma unroll
        for (int nt = 0; nt < 4; nt++) acc[nt] -= mu * cs[nt];
        ldswf4(sm + XS + w * 2048 + lane * 16, kh ? acc[0] : acc[2]);
        ldswf4(sm + XS + w * 2048 + 1024 + lane * 16, kh ? acc[1] : acc[3]);
        bar();
        f32x4 v0 = kh ? acc[2] : acc[0], v1 = kh ? acc[3] : acc[1];
        v0 += ldsrf4(sm + XS + (w ^ 4) * 2048 + lane * 16);
        v1 += ldsrf4(sm + XS + (w ^ 4) * 2048 + 1024 + lane * 16);
        STAMP(31);
        float y[8];
#pragma unroll
        for (int i = 0; i < 4; i++) { y[i] = gelu(v0[i] * r2); y[4 + i] = gelu(v1[i] * r2); }
#pragma unroll
        for (int i = 0; i < 8; i++) xres1[i] = (hres[i] - mu) * r;
        u32x4 hv = {pk2(y[0], y[1]), pk2(y[2], y[3]), pk2(y[4], y[5]), pk2(y[6], y[7])};
        st_sc1(hv, rws, (((2 * j + kh) * 4 + g4) * 2048 + tokq) * 16, OFF_HH);
        dma_w128<8>(w3 + (size_t)(64 * w) * 3584 + j * 448, 3584, sm + R1 + w * 8192, lane);
        STAMP(32);
        waitvm<8>();
        if (lane == 0) flag_store(ws, OFF_FHH + (Tq * 16 + 2 * j + kh) * 8, epoch);
    }
    STAMP(10);
    {
        const int r0 = 4 * g4 + lq;
        unsigned wa[4];
#pragma unroll
        for (int nt = 0; nt < 4; nt++) wa[nt] = opq(sm + R0 + (256 * kh + r0) * 128 + ((nt ^ swz128(r0)) * 32) + lp * 8);
        {
            const int row = Tq - 1 + (lane >> 3);
            const bool need = lane < 24 && row >= 0 && row < 128;
            poll_flags(ws, OFF_FHH + (row * 16 + 8 * kh + (lane & 7)) * 8, need, epoch, dead);
        }
        STAMP(11);
        const int dlt = kh ? 2 : 1;
        const int ta = tokq - dlt, tb = tokq + dlt;
        const unsigned va = ta >= 0 ? (g4 * 2048 + ta) * 16 : 0x80000000u;
        const unsigned vb = tb < 2048 ? (g4 * 2048 + tb) * 16 : 0x80000000u;
        u32x4 bf[8];
#pragma unroll
        for (int s = 0; s < 8; s++) bf[s] = ld_sc1(rws, s < 4 ? va : vb, OFF_HH + (8 * kh + s) * 4 * 2048 * 16);
        f32x4 acc[4] = {};
        bar();
        gemm_p<8, 16 * 128, 4096, 4, 1>(wa, bf, acc);
        STAMP(33);
        ldswf4(sm + XS + w * 2048 + lane * 16, kh ? acc[0] : acc[2]);
        ldswf4(sm + XS + w * 2048 + 1024 + lane * 16, kh ? acc[1] : acc[3]);
        bar();
        f32x4 v0 = kh ? acc[2] : acc[0], v1 = kh ? acc[3] : acc[1];
        v0 += ldsrf4(sm + XS + (w ^ 4) * 2048 + lane * 16);
        v1 += ldsrf4(sm + XS + (w ^ 4) * 2048 + 1024 + lane * 16);
#pragma unroll
        for (int i = 0; i < 4; i++) { xres1[i] += v0[i]; xres1[4 + i] += v1[i]; }
        STAMP(34);
        float mean, m2; piece_stats(xres1, mean, m2);
        u32x4 hv = {pk2(xres1[0], xres1[1]), pk2(xres1[2], xres1[3]), pk2(xres1[4], xres1[5]), pk2(xres1[6], xres1[7])};
        st_sc1(hv, rws, (((2 * j + kh) * 4 + g4) * 2048 + tokq) * 16, OFF_X1);
        if (g4 == 0) st_sc1_64(u32x2{fbits(mean), fbits(m2)}, rws, ((2 * j + kh) * 2048 + tokq) * 8, OFF_XS);
        dma_w128<8>(w3 + (size_t)(64 * w) * 3584 + j * 448 + 128, 3584, sm + R0 + w * 8192, lane);
        STAMP(35);
        waitvm<8>();
        if (lane == 0) flag_store(ws, OFF_FX1 + (Tq * 16 + 2 * j + kh) * 8, epoch);
    }
    STAMP(12);
    {
        const int nh = kh;
        const int r0 = 4 * g4 + lq;
        constexpr unsigned CSB = R2 + 16384;
        unsigned wl[2];
#pragma unroll
        for (int i = 0; i < 2; i++) wl[i] = r0 * 128 + (((2 * nh + i) ^ swz128(r0)) * 32) + lp * 8;
        const unsigned wl3 = r0 * 64 + ((nh ^ ((r0 >> 2) & 1)) * 32) + lp * 8;
        waitvm<0>();
        bar();
        STAMP(36);
        auto cs_part = [&](unsigned region, int pass) {
            unsigned wa[2] = {opq(sm + region + wl[0] + tq * 16384), opq(sm + region + wl[1] + tq * 16384)};
            f32x4 c[2] = {};
            gemm_ones<4, 2048, 4096, 2, 2>(wa, c);
            if (t16 == 0) {
                ldswf4(sm + CSB + (((4 * pass + 2 * nh) * 4 + tq) * 16 + 4 * g4) * 4, c[0]);
                ldswf4(sm + CSB + (((4 * pass + 2 * nh + 1) * 4 + tq) * 16 + 4 * g4) * 4, c[1]);
            }
        };
        cs_part(R1, 0);
        cs_part(R0, 1);
        STAMP(37);
        poll_flags(ws, OFF_FX1 + (Tq * 16 + (lane & 15)) * 8, lane < 16, epoch, dead);
        STAMP(13);
        u32x4 bf[16];
#pragma unroll
        for (int s = 0; s < 16; s++) bf[s] = ld_sc1(rws, (g4 * 2048 + tokq) * 16, OFF_X1 + s * 4 * 2048 * 16);
        float mu, var; combine_stats(rws, OFF_XS, tokq, g4, mu, var);
        const float r = __builtin_amdgcn_rsqf(var + 1e-5f);
        auto cs_get = [&](int tile) {
            f32x4 c = ldsrf4(sm + CSB + ((tile * 4 + 0) * 16 + 4 * g4) * 4);
#pragma unroll
            for (int q = 1; q < 4; q++) c += ldsrf4(sm + CSB + ((tile * 4 + q) * 16 + 4 * g4) * 4);
            return c;
        };
        auto epi = [&](const f32x4 (&a)[2], int pass) {
            const f32x4 c0 = cs_get(4 * pass + 2 * nh), c1 = cs_get(4 * pass + 2 * nh + 1);
            float y[8];
#pragma unroll
            for (int i = 0; i < 4; i++) { y[i] = gelu((a[0][i] - mu * c0[i]) * r); y[4 + i] = gelu((a[1][i] - mu * c1[i]) * r); }
            u32x4 gv = {pk2(y[0], y[1]), pk2(y[2], y[3]), pk2(y[4], y[5]), pk2(y[6], y[7])};
            st_sc1(gv, rws, (((7 * j + 2 * pass + nh) * 4 + g4) * 2048 + tokq) * 16, OFF_G);
        };
        auto main_pass = [&](unsigned region, f32x4 (&a)[2]) {
            unsigned wa[2] = {opq(sm + region + wl[0]), opq(sm + region + wl[1])};
            gemm_p<16, 2048, 4096, 2, 2>(wa, bf, a);
        };
        f32x4 a[2] = {};
        STAMP(38);
        main_pass(R1, a);
        STAMP(39);
        bar();
        dma_w128<8>(w3 + (size_t)(64 * w) * 3584 + j * 448 + 256, 3584, sm + R1 + w * 8192, lane);
        epi(a, 0);
        STAMP(40);
        a[0] = f32x4{}; a[1] = f32x4{};
        main_pass(R0, a);
        STAMP(41);
        waitvm<0>();
        if (lane == 0) flag_store(ws, OFF_FG + ((0 * 128 + Tq) * 16 + 2 * j + nh) * 8, epoch);
        bar();
        {
            const int rr = lane >> 2, c = lane & 3;
            const unsigned voff = rr * 3584 + ((((c >> 1) ^ ((rr >> 2) & 1)) << 1) | (c & 1)) * 16;
#pragma unroll
            for (int i = 0; i < 4; i++) dma16(w3 + (size_t)(64 * w + 16 * i) * 3584 + j * 448 + 384, voff, sm + R0 + w * 4096 + i * 1024);
        }
        epi(a, 1);
        STAMP(42);
        cs_part(R1, 2);
        a[0] = f32x4{}; a[1] = f32x4{};
        main_pass(R1, a);
        STAMP(43);
        waitvm<0>();
        if (lane == 0) flag_store(ws, OFF_FG + ((1 * 128 + Tq) * 16 + 2 * j + nh) * 8, epoch);
        bar();
        dma_w128<8>(w4 + (size_t)(224 * w) * 1024 + j * 128, 1024, sm + R1 + w * 8192, lane);
        epi(a, 2);
        STAMP(44);
        {
            unsigned wc[1] = {opq(sm + R0 + wl3 + tq * 8192)};
            f32x4 c[1] = {};
            gemm_ones<4, 1024, 2048, 1, 3>(wc, c);
            if (t16 == 0) ldswf4(sm + CSB + (((12 + nh) * 4 + tq) * 16 + 4 * g4) * 4, c[0]);
            unsigned wa[1] = {opq(sm + R0 + wl3)};
            f32x4 a0[1] = {};
            gemm_p<16, 1024, 2048, 1, 4>(wa, bf, a0);
            STAMP(45);
            waitvm<0>();
            if (lane == 0) flag_store(ws, OFF_FG + ((2 * 128 + Tq) * 16 + 2 * j + nh) * 8, epoch);
            bar();
            const f32x4 c0 = cs_get(12 + nh);
            float y[4];
#pragma unroll
            for (int i = 0; i < 4; i++) y[i] = gelu((a0[0][i] - mu * c0[i]) * r);
            st_sc1_64(u32x2{pk2(y[0], y[1]), pk2(y[2], y[3])}, rws, (((7 * j + 6) * 4 + g4) * 2048 + tokq) * 16 + nh * 8, OFF_G);
            waitvm<0>();
            if (lane == 0) flag_store(ws, OFF_FG + ((3 * 128 + Tq) * 16 + 2 * j + nh) * 8, epoch);
        }
    }
    STAMP(14);
    {
        const int r0 = 4 * g4 + lq;
        unsigned wl[4];
#pragma unroll
        for (int nt = 0; nt < 4; nt++) wl[nt] = r0 * 128 + ((nt ^ swz128(r0)) * 32) + lp * 8;
        f32x4 acc[4] = {};
        u32x4 bfa[8], bfb[8];
        auto fetch = [&](int pass, u32x4 (&bf)[8]) {
            poll_flags(ws, OFF_FG + ((pass * 128 + Tq) * 16 + 8 * kh + (lane & 7)) * 8, lane < 8, epoch, dead);
            if (pass < 3) {
#pragma unroll
                for (int s = 0; s < 8; s++) bf[s] = ld_sc1(rws, (g4 * 2048 + tokq) * 16, OFF_G + (7 * (4 * kh + (s >> 1)) + 2 * pass + (s & 1)) * 4 * 2048 * 16);
            } else {
#pragma unroll
                for (int s = 0; s < 4; s++) bf[s] = ld_sc1(rws, (g4 * 2048 + tokq) * 16, OFF_G + (7 * (4 * kh + s) + 6) * 4 * 2048 * 16);
            }
        };
        auto landed = [&](u32x4 (&bf)[8], int n) {
            waitvm<0>();
#pragma unroll
            for (int s = 0; s < 8; s++) if (s < n) asm volatile("" : "+v"(bf[s]));
        };
        auto chunk = [&](unsigned region, int koff, const u32x4 (&bf)[8], bool full) {
            unsigned wa[4];
#pragma unroll
            for (int nt = 0; nt < 4; nt++) wa[nt] = opq(sm + region + koff + wl[nt]);
            if (full) gemm_p<8, 16 * 128, 4096, 4, 1>(wa, bf, acc);
            else gemm_p<4, 16 * 128, 4096, 4, 1>(wa, bf, acc);
        };
        fetch(0, bfa);
        fetch(1, bfb);
        asm volatile("" : "+v"(bfa[0]));
        dma_w128<8>(w4 + (size_t)(224 * w + 64) * 1024 + j * 128, 1024, sm + R0 + w * 8192, lane);
        STAMP(48);
        chunk(R1, 256 * kh * 128, bfa, true);
        STAMP(49);
        landed(bfb, 8);
        bar();
        fetch(2, bfa);
        dma_w128<8>(w4 + (size_t)(224 * w + 128) * 1024 + j * 128, 1024, sm + R1 + w * 8192, lane);
        STAMP(50);
        chunk(R0, 256 * kh * 128, bfb, true);
        STAMP(51);
        landed(bfa, 8);
        bar();
        dma_w128<4>(w4 + (size_t)(224 * w + 192) * 1024 + j * 128, 1024, sm + R0 + w * 4096, lane);
        STAMP(52);
        chunk(R1, 256 * kh * 128, bfa, true);
        fetch(3, bfb);
        STAMP(53);
        landed(bfb, 4);
        bar();
        chunk(R0, 128 * kh * 128, bfb, false);
        STAMP(15);
        ldswf4(sm + XS + w * 2048 + lane * 16, kh ? acc[0] : acc[2]);
        ldswf4(sm + XS + w * 2048 + 1024 + lane * 16, kh ? acc[1] : acc[3]);
        bar();
        f32x4 v0 = kh ? acc[2] : acc[0], v1 = kh ? acc[3] : acc[1];
        v0 += ldsrf4(sm + XS + (w ^ 4) * 2048 + lane * 16);
        v1 += ldsrf4(sm + XS + (w ^ 4) * 2048 + 1024 + lane * 16);
        u32x2 o0 = {pk2(hres[0] + xres1[0] + v0[0], hres[1] + xres1[1] + v0[1]), pk2(hres[2] + xres1[2] + v0[2], hres[3] + xres1[3] + v0[3])};
        u32x2 o1 = {pk2(hres[4] + xres1[4] + v1[0], hres[5] + xres1[5] + v1[1]), pk2(hres[6] + xres1[6] + v1[2], hres[7] + xres1[7] + v1[3])};
        *(GASP u32x2*)(out + (size_t)tokq * 1024 + (64 * j + 32 * kh + 4 * g4) * 2) = o0;
        *(GASP u32x2*)(out + (size_t)tokq * 1024 + (64 * j + 32 * kh + 16 + 4 * g4) * 2) = o1;
    }
    STAMP(16); CSTAMP(63);
    STAMP_FLUSH();
}

extern "C" void kernel_launch(void* const* d_in, const int* in_sizes, int n_in, void* d_out, int out_size, void* d_ws, size_t ws_size,
                              hipStream_t stream) {
    (void)in_sizes; (void)n_in; (void)out_size; (void)ws_size;
    hipFuncSetAttribute((const void*)fused_block_kernel, hipFuncAttributeMaxDynamicSharedMemorySize, LDS_BYTES);
    fused_block_kernel<<<256, 512, LDS_BYTES, stream>>>((const char*)d_in[0], (const char*)d_in[1], (const char*)d_in[2], (const char*)d_in[3],
                                                        (const char*)d_in[4], (const char*)d_in[6], (const char*)d_in[7], (const char*)d_in[8],
                                                        (const char*)d_in[9], (char*)d_out, (char*)d_ws);
}
```

```cpp
#include <hip/hip_runtime.h>
#include <stdint.h>

typedef float f32x2 __attribute__((ext_vector_type(2)));
typedef float f32x4 __attribute__((ext_vector_type(4)));
typedef float f32x8 __attribute__((ext_vector_type(8)));
typedef float f32x16 __attribute__((ext_vector_type(16)));
typedef __bf16 bf16x2 __attribute__((ext_vector_type(2)));
typedef __bf16 bf16x8 __attribute__((ext_vector_type(8)));
typedef __bf16 bf16x16 __attribute__((ext_vector_type(16)));
typedef unsigned u32x2 __attribute__((ext_vector_type(2)));
typedef unsigned u32x4 __attribute__((ext_vector_type(4)));
typedef short s16x4 __attribute__((ext_vector_type(4)));
typedef unsigned long long u64;
#define LDSP __attribute__((address_space(3)))
#define GASP __attribute__((address_space(1)))
#define DEVI __device__ __forceinline__

constexpr unsigned OFF_E   = 0;
constexpr unsigned OFF_KVF = 4096;
constexpr unsigned OFF_CLR = 8192;
constexpr unsigned OFF_KI  = 0x40000;
constexpr unsigned OFF_VI  = OFF_KI + 0x10000;
constexpr unsigned OFF_HS  = 0x80000;
constexpr unsigned OFF_XS  = 0xC0000;
constexpr unsigned OFF_O   = 0x100000;
constexpr unsigned OFF_H   = 0x300000;
constexpr unsigned OFF_HH  = 0x500000;
constexpr unsigned OFF_X1  = 0x700000;
constexpr unsigned OFF_G   = 0x900000;
constexpr unsigned OFF_DBG = 0x1000000;
constexpr unsigned OFF_W3T = 0x1100000;
constexpr unsigned OFF_W4T = 0x1300000;
constexpr unsigned WT_J = 228 * 1024;
constexpr unsigned WS_USED = 0x1500000;

constexpr unsigned R0 = 0, R1 = 65536, R2 = 131072;
constexpr unsigned LDS_BYTES = 163840;

DEVI unsigned pk2(float a, float b) { f32x2 f = {a, b}; return __builtin_bit_cast(unsigned, __builtin_convertvector(f, bf16x2)); }
DEVI float bflo(unsigned u) { return __builtin_bit_cast(float, u << 16); }
DEVI float bfhi(unsigned u) { return __builtin_bit_cast(float, u & 0xffff0000u); }
DEVI float dot2(unsigned a, unsigned b, float c) { return __builtin_amdgcn_fdot2_f32_bf16(__builtin_bit_cast(bf16x2, a), __builtin_bit_cast(bf16x2, b), c, false); }
DEVI unsigned fbits(float f) { return __builtin_bit_cast(unsigned, f); }
DEVI float bitsf(unsigned u) { return __builtin_bit_cast(float, u); }
DEVI float x32sum(float v) { auto r = __builtin_amdgcn_permlane32_swap(fbits(v), fbits(v), false, false); return bitsf(r[0]) + bitsf(r[1]); }
DEVI float x16sum(float v) { auto r = __builtin_amdgcn_permlane16_swap(fbits(v), fbits(v), false, false); return bitsf(r[0]) + bitsf(r[1]); }
DEVI float x32max(float v) { auto r = __builtin_amdgcn_permlane32_swap(fbits(v), fbits(v), false, false); return fmaxf(bitsf(r[0]), bitsf(r[1])); }
DEVI float x16max(float v) { auto r = __builtin_amdgcn_permlane16_swap(fbits(v), fbits(v), false, false); return fmaxf(bitsf(r[0]), bitsf(r[1])); }
template <int C, int RM> DEVI float dppf(float v) { return __builtin_bit_cast(float, __builtin_amdgcn_update_dpp(0, __builtin_bit_cast(int, v), C, RM, 0xf, true)); }
DEVI float wave_sum(float v) {
    v += dppf<0x111, 0xf>(v); v += dppf<0x112, 0xf>(v); v += dppf<0x114, 0xf>(v); v += dppf<0x118, 0xf>(v);
    v += dppf<0x142, 0xa>(v); v += dppf<0x143, 0xc>(v);
    return __builtin_bit_cast(float, __builtin_amdgcn_readlane(__builtin_bit_cast(int, v), 63));
}
DEVI float wave_max_nn(float v) {
    v = fmaxf(v, dppf<0x111, 0xf>(v)); v = fmaxf(v, dppf<0x112, 0xf>(v)); v = fmaxf(v, dppf<0x114, 0xf>(v)); v = fmaxf(v, dppf<0x118, 0xf>(v));
    v = fmaxf(v, dppf<0x142, 0xa>(v)); v = fmaxf(v, dppf<0x143, 0xc>(v));
    return __builtin_bit_cast(float, __builtin_amdgcn_readlane(__builtin_bit_cast(int, v), 63));
}
DEVI void bar() { asm volatile("s_waitcnt lgkmcnt(0)\n\ts_barrier" ::: "memory"); }
template <int N> DEVI void waitvm() { asm volatile("s_waitcnt vmcnt(%0)" ::"n"(N) : "memory"); }
#define SBAR() __builtin_amdgcn_sched_barrier(0)

DEVI void dma16(const void* sbase, unsigned voff, unsigned lds) {
    unsigned keep;
    asm volatile("s_mov_b32 %0, m0\n\ts_mov_b32 m0, %3\n\ts_nop 2\n\tglobal_load_lds_dwordx4 %1, %2\n\ts_mov_b32 m0, %0"
                 : "=&s"(keep) : "v"(voff), "s"(sbase), "s"(lds) : "memory");
}
DEVI void dma16x4(const void* sbase, unsigned v0, unsigned v1, unsigned v2, unsigned v3, unsigned lds) {
    unsigned keep;
    asm volatile("s_mov_b32 %0, m0\n\ts_mov_b32 m0, %6\n\ts_nop 0\n\tglobal_load_lds_dwordx4 %1, %5\n\tglobal_load_lds_dwordx4 %2, %5 offset:1024\n\t"
                 "global_load_lds_dwordx4 %3, %5 offset:2048\n\tglobal_load_lds_dwordx4 %4, %5 offset:3072\n\ts_mov_b32 m0, %0"
                 : "=&s"(keep) : "v"(v0), "v"(v1), "v"(v2), "v"(v3), "s"(sbase), "s"(lds) : "memory");
}
DEVI void dma16s(const void* sbase, unsigned voff, unsigned lds) {
    unsigned keep;
    asm volatile("s_mov_b32 %0, m0\n\ts_mov_b32 m0, %3\n\ts_nop 2\n\tglobal_load_lds_dwordx4 %1, %2 sc1\n\ts_mov_b32 m0, %0"
                 : "=&s"(keep) : "v"(voff), "s"(sbase), "s"(lds) : "memory");
}
template <int AUX = 16> DEVI void dma16c(const char* src, unsigned lds) { __builtin_amdgcn_global_load_lds((const GASP void*)src, (LDSP void*)lds, 16, 0, AUX); }
DEVI bf16x8 trfrag(unsigned a0, unsigned a1) {
    s16x4 lo = __builtin_amdgcn_ds_read_tr16_b64_v4i16((LDSP s16x4*)a0);
    s16x4 hi = __builtin_amdgcn_ds_read_tr16_b64_v4i16((LDSP s16x4*)a1);
    return __builtin_bit_cast(bf16x8, __builtin_shufflevector(lo, hi, 0, 1, 2, 3, 4, 5, 6, 7));
}
DEVI u32x4 ldsr128(unsigned a) { return *(LDSP u32x4*)a; }
DEVI void ldsw128(unsigned a, u32x4 v) { *(LDSP u32x4*)a = v; }
DEVI f32x4 ldsrf4(unsigned a) { return *(LDSP f32x4*)a; }
DEVI void ldswf4(unsigned a, f32x4 v) { *(LDSP f32x4*)a = v; }
typedef __amdgpu_buffer_rsrc_t rsrc_t;
DEVI rsrc_t mkrsrc(const void* p, unsigned bytes) { return __builtin_amdgcn_make_buffer_rsrc((void*)p, 0, bytes, 0x00020000); }
#define LAUX 16
#define SAUX 16
DEVI u32x4 ld_sc1(rsrc_t r, unsigned voff, unsigned soff) { return __builtin_amdgcn_raw_buffer_load_b128(r, voff, soff, LAUX); }
template <int AUX> DEVI u32x4 ld_aux(rsrc_t r, unsigned voff, unsigned soff) { return __builtin_amdgcn_raw_buffer_load_b128(r, voff, soff, AUX); }
DEVI u32x2 ld_sc1_64(rsrc_t r, unsigned voff, unsigned soff) { return __builtin_amdgcn_raw_buffer_load_b64(r, voff, soff, LAUX); }
DEVI void st_sc1(u32x4 v, rsrc_t r, unsigned voff, unsigned soff) {
    __builtin_amdgcn_raw_buffer_store_b128(v, r, voff, soff, SAUX);
    asm volatile("s_nop 1" :: "v"(v));
}
DEVI void st_sc1_64(u32x2 v, rsrc_t r, unsigned voff, unsigned soff) { __builtin_amdgcn_raw_buffer_store_b64(v, r, voff, soff, SAUX); }
DEVI void flag_store(char* ws, unsigned off, u64 v) { __hip_atomic_store((GASP u64*)(ws + off), v, __ATOMIC_RELAXED, __HIP_MEMORY_SCOPE_AGENT); }
DEVI u64 flag_load(char* ws, unsigned off) { return __hip_atomic_load((GASP u64*)(ws + off), __ATOMIC_RELAXED, __HIP_MEMORY_SCOPE_AGENT); }
constexpr int SPIN_MAX = 40000;
DEVI void poll_flags(char* ws, unsigned off, bool need, u64 tag, int& dead) {
    if (dead) return;
    for (int it = 0; it < SPIN_MAX; it++) {
        u64 v = need ? flag_load(ws, off) : tag;
        if (__all(v == tag)) { asm volatile("" ::: "memory"); return; }
        __builtin_amdgcn_s_sleep(1);
        asm volatile("" ::: "memory");
    }
    dead = 1;
}
DEVI float gelu(float x) {
    float z = x * fmaf(x * x, -0.10294324f, -2.3022082f);
    float e = __builtin_amdgcn_exp2f(z);
    return x * __builtin_amdgcn_rcpf(1.f + e);
}
DEVI f32x2 gelu2(f32x2 x, f32x2 cc) {
    const f32x2 c1 = {cc[0], cc[0]}, c2 = {cc[1], cc[1]};
    const f32x2 z = x * __builtin_elementwise_fma(x * x, c1, c2);
    const f32x2 d = f32x2{__builtin_amdgcn_exp2f(z[0]), __builtin_amdgcn_exp2f(z[1])} + f32x2{1.f, 1.f};
    return x * f32x2{__builtin_amdgcn_rcpf(d[0]), __builtin_amdgcn_rcpf(d[1])};
}
DEVI f32x4 mfma16(bf16x8 a, bf16x8 b, f32x4 c) { return __builtin_amdgcn_mfma_f32_16x16x32_bf16(a, b, c, 0, 0, 0); }
DEVI f32x16 mfma32(bf16x8 a, bf16x8 b, f32x16 c) { return __builtin_amdgcn_mfma_f32_32x32x16_bf16(a, b, c, 0, 0, 0); }
DEVI bf16x8 asbf(u32x4 v) { return __builtin_bit_cast(bf16x8, v); }
template <bool B> struct BoolC { static constexpr bool value = B; };
DEVI int swz128(int row) { return (((row >> 1) & 1) << 1) | ((row >> 2) & 1); }

#define PROBE 0
#define PA 0
#define PB 16
#define AMP 4
#define PBLK 100
#define PW 4
#define PMIN 0
#define PCLK 0
#define PLAT 0
#define PSEG 0
#define CPFL 16
#define CPFX 0
#define STAMP(i) do {} while (0)
#define CSTAMP(i) do {} while (0)
#define STAMP_DECL do {} while (0)
#define STAMP_FLUSH() do {} while (0)

template <int NI>
DEVI void dma_w128(const char* src, unsigned stride, unsigned lds, int lane) {
    const int r = lane >> 3, c = lane & 7;
    const unsigned voff = r * stride + ((((c >> 1) ^ swz128(r)) << 1) | (c & 1)) * 16;
#pragma unroll
    for (int i = 0; i < NI; i++) dma16(src + (size_t)i * 8 * stride, voff, lds + i * 1024);
}

DEVI f32x16 pvmac(bf16x8 v, const f32x16& s, int half, f32x16 o) {
    f32x8 p = half == 0 ? __builtin_shufflevector(s, s, 0, 1, 2, 3, 4, 5, 6, 7) : __builtin_shufflevector(s, s, 8, 9, 10, 11, 12, 13, 14, 15);
    return __builtin_amdgcn_smfmac_f32_32x32x32_bf16(v, __builtin_bit_cast(bf16x16, p), o, 0xDDDDDDDD, 0, 0);
}
template <int A, int B> DEVI void exps(f32x16& s) {
#pragma unroll
    for (int i = A; i < B; i++) s[i] = __builtin_amdgcn_exp2f(s[i]);
}

DEVI unsigned opq(unsigned a) { asm volatile("" : "+v"(a)); return a; }
template <int NS, int D2, int ST, int NT, int DEPTH, class SIDE>
DEVI void gemm_p(const unsigned (&wa)[NT], const u32x4* bf, f32x4 (&acc)[NT], SIDE side) {
    bf16x8 fr[DEPTH + 1][NT];
#pragma unroll
    for (int d = 0; d < DEPTH; d++)
#pragma unroll
        for (int nt = 0; nt < NT; nt++) fr[d][nt] = trfrag(wa[nt] + d * ST, wa[nt] + d * ST + D2);
#pragma unroll
    for (int s = 0; s < NS; s++) {
        SBAR();
        if (s + DEPTH < NS) {
#pragma unroll
            for (int nt = 0; nt < NT; nt++) fr[(s + DEPTH) % (DEPTH + 1)][nt] = trfrag(wa[nt] + (s + DEPTH) * ST, wa[nt] + (s + DEPTH) * ST + D2);
        }
        SBAR();
        side(s);
        SBAR();
#pragma unroll
        for (int nt = 0; nt < NT; nt++) acc[nt] = mfma16(fr[s % (DEPTH + 1)][nt], asbf(bf[s]), acc[nt]);
    }
    SBAR();
}
#define GRING 0
template <int NS, int D2, int ST, int NT, int RING, class SIDE>
DEVI void gemm_r(const unsigned (&wa)[NT], const u32x4* bf, f32x4 (&acc)[NT], SIDE side) {
    bf16x8 fr[RING + 1];
    constexpr int N = NS * NT;
#pragma unroll
    for (int d = 0; d < RING; d++) fr[d] = trfrag(wa[d % NT] + (d / NT) * ST, wa[d % NT] + (d / NT) * ST + D2);
#pragma unroll
    for (int i = 0; i < N; i++) {
        SBAR();
        if (i + RING < N) { const int n = i + RING; fr[n % (RING + 1)] = trfrag(wa[n % NT] + (n / NT) * ST, wa[n % NT] + (n / NT) * ST + D2); }
        if (i % NT == 0) side(i / NT);
        SBAR();
        acc[i % NT] = mfma16(fr[i % (RING + 1)], asbf(bf[i / NT]), acc[i % NT]);
    }
    SBAR();
}
constexpr unsigned EMPTY = 0xFFFFFFFFu;
DEVI unsigned san(unsigned v) { return v < 0xFFFFFFFEu ? v : 0xFFFFFFFEu; }
DEVI unsigned umax(unsigned a, unsigned b) { return a > b ? a : b; }
DEVI unsigned max4(u32x4 v) { return umax(umax(v[0], v[1]), umax(v[2], v[3])); }
DEVI u32x4 pack8(const float (&v)[8]) { return u32x4{san(pk2(v[0], v[1])), san(pk2(v[2], v[3])), san(pk2(v[4], v[5])), san(pk2(v[6], v[7]))}; }
DEVI bool anyempty(unsigned m) { return __builtin_amdgcn_ballot_w64(m == EMPTY) != 0; }
#define VSLEEP 4
template <int N, class LD>
DEVI void verify(u32x4 (&bf)[N], LD ld, int& dead) {
    unsigned m = max4(bf[0]);
#pragma unroll
    for (int s = 1; s < N; s++) m = umax(umax(m, umax(bf[s][0], bf[s][1])), umax(bf[s][2], bf[s][3]));
    if (!anyempty(m) || dead) return;
    for (int it = 0; it < SPIN_MAX; it++) {
        bool pend = false;
        if (VSLEEP) __builtin_amdgcn_s_sleep(VSLEEP);
        asm volatile("" ::: "memory");
#pragma unroll
        for (int s = 0; s < N; s++) {
            if (anyempty(max4(bf[s]))) { bf[s] = ld(s); pend = true; }
        }
        if (!pend) return;
    }
    dead = 1;
}
template <class LD>
DEVI void verify_st(u32x2 (&p)[4], LD ld, int& dead) {
    for (int it = 0; it < SPIN_MAX; it++) {
        const unsigned m = umax(umax(umax(p[0][0], p[0][1]), umax(p[1][0], p[1][1])), umax(umax(p[2][0], p[2][1]), umax(p[3][0], p[3][1])));
        if (!anyempty(m) || dead) return;
        asm volatile("" ::: "memory");
#pragma unroll
        for (int i = 0; i < 4; i++) p[i] = ld(i);
    }
    dead = 1;
}
DEVI void lds_signal(unsigned addr, int lane) {
    asm volatile("s_waitcnt lgkmcnt(0)" ::: "memory");
    if (lane == 0) *(volatile LDSP unsigned*)addr = 1u;
}
template <int SL = 1>
DEVI void lds_wait4(unsigned addr, int& dead) {
    if (dead) return;
    for (int it = 0; it < SPIN_MAX; it++) {
        const u32x4 v = *(volatile LDSP u32x4*)addr;
        if (__builtin_amdgcn_readfirstlane(v[0] & v[1] & v[2] & v[3])) { asm volatile("" ::: "memory"); return; }
        if (SL) __builtin_amdgcn_s_sleep(SL);
    }
    dead = 1;
}

template <int SL = 1>
DEVI void lds_wait8(unsigned addr, int& dead) {
    if (dead) return;
    for (int it = 0; it < SPIN_MAX; it++) {
        const u32x4 v = *(volatile LDSP u32x4*)addr, u = *(volatile LDSP u32x4*)(addr + 16);
        if (__builtin_amdgcn_readfirstlane(v[0] & v[1] & v[2] & v[3] & u[0] & u[1] & u[2] & u[3])) { asm volatile("" ::: "memory"); return; }
        if (SL) __builtin_amdgcn_s_sleep(SL);
    }
    dead = 1;
}

DEVI void combine_stats(const u32x2 (&p)[4], float& mu, float& var) {
    float sm = bitsf(p[0][0]) + bitsf(p[1][0]) + bitsf(p[2][0]) + bitsf(p[3][0]);
    sm = x32sum(x16sum(sm));
    mu = sm * (1.f / 16.f);
    float m2 = 0.f, dv = 0.f;
#pragma unroll
    for (int i = 0; i < 4; i++) { m2 += bitsf(p[i][1]); float d = bitsf(p[i][0]) - mu; dv = fmaf(d, d, dv); }
    m2 = fmaf(dv, 32.f, m2);
    m2 = x32sum(x16sum(m2));
    var = m2 * (1.f / 512.f);
}
DEVI void piece_stats(const float (&v)[8], float& mean, float& m2) {
    float s = ((v[0] + v[1]) + (v[2] + v[3])) + ((v[4] + v[5]) + (v[6] + v[7]));
    s = x32sum(x16sum(s));
    mean = s * (1.f / 32.f);
    float q = 0.f;
#pragma unroll
    for (int i = 0; i < 8; i++) { float d = v[i] - mean; q = fmaf(d, d, q); }
    m2 = x32sum(x16sum(q));
}

#define AT_E(b, i) "v_exp_f32 v[" #b "+" #i "], v[" #b "+" #i "]\n\t"
#define AT_E5(b, i) AT_E(b, i) AT_E(b, i + 1) AT_E(b, i + 2) AT_E(b, i + 3) AT_E(b, i + 4)
#define AT_QK(b, K, Q) "v_mfma_f32_32x32x16_bf16 v[" #b ":" #b "+15], %[" #K "], %[" #Q "], 0\n\t"
#define AT_PV(O, V, b) "v_smfmac_f32_32x32x32_bf16 %[" #O "], %[" #V "], v[" #b ":" #b "+7], %[idx]\n\t"
#define AT_UNIT(b, bn, bp, O, Op, K, Qn, V0, V1p, PRE, MEM) \
    AT_E(b, 0) PRE AT_QK(bn, K, Qn) AT_E5(b, 1) AT_PV(Op, V1p, bp + 8) MEM AT_E5(b, 6) AT_PV(O, V0, b) AT_E5(b, 11)
#define AT_TILE(K, Kn, V0, V1, V1p, MEM, WAIT) \
    AT_UNIT(192, 208, 240, o0, o3, K, q1, V0, V1p, "", MEM) \
    AT_UNIT(208, 224, 192, o1, o0, K, q2, V0, V1, "", "") \
    AT_UNIT(224, 240, 208, o2, o1, K, q3, V0, V1, "", "") \
    AT_UNIT(240, 192, 224, o3, o2, Kn, q0, V0, V1, WAIT, "")
#define AT_LD(D, A, S, OFF) "buffer_load_dwordx4 %[" #D "], %[" #A "], %[rs], %[" #S "] offen offset:" #OFF " sc1\n\t"
#define AT_ST(A, S) "buffer_store_dwordx4 %[e4], %[" #A "], %[rs], %[" #S "] offen sc1\n\t"
#define AT_NOST(A, S) ""
#define AT_DMA(M, P) "s_mov_b32 m0, %[" #M "]\n\ts_nop 0\n\t" \
    "global_load_lds_dwordx4 %[r0], " P "\n\tglobal_load_lds_dwordx4 %[r1], " P " offset:1024\n\t" \
    "global_load_lds_dwordx4 %[r2], " P " offset:2048\n\tglobal_load_lds_dwordx4 %[r3], " P " offset:3072\n\t"
#define AT_LOOP(ST, WAIT0, WAIT1, DMA1, DMA3, EXTRA_PRE, EXTRA_POST) \
    "s_mov_b32 %[cnt], 4\n" \
    "1:\n\t" \
    EXTRA_PRE \
    "s_cmp_eq_u32 %[cnt], 1\n\t" \
    "s_cselect_b32 %[msk], 0x80000000, 0\n\t" \
    "v_or_b32 %[kl2], %[msk], %[kl]\n\t" \
    "v_or_b32 %[vl2], %[msk], %[vl]\n\t" \
    AT_TILE(k0, k1, v00, v10, v13, AT_LD(k3, kl, sk, 0) AT_LD(v03, vl, sv, 0) AT_LD(v13, vl, sv, 768) ST(fo, st0), WAIT0) \
    AT_TILE(k1, k2, v01, v11, v10, AT_LD(k0, kl2, sk, 1024) AT_LD(v00, vl2, sv, 1536) AT_LD(v10, vl2, sv, 2304) ST(fo, st1) DMA1, WAIT1) \
    AT_TILE(k2, k3, v02, v12, v11, AT_LD(k1, kl2, sk, 2048) AT_LD(v01, vl2, sw, 0) AT_LD(v11, vl2, sw, 768) ST(fo, st2), WAIT0) \
    AT_TILE(k3, k0, v03, v13, v12, AT_LD(k2, kl2, sk, 3072) AT_LD(v02, vl2, sw, 1536) AT_LD(v12, vl2, sw, 2304) ST(fo2, st3) DMA3, WAIT1) \
    "s_add_u32 %[sk], %[sk], 4096\n\t" \
    "s_add_u32 %[sv], %[sv], 6144\n\t" \
    "s_add_u32 %[sw], %[sw], 6144\n\t" \
    EXTRA_POST \
    "s_sub_u32 %[cnt], %[cnt], 1\n\t" \
    "s_cmp_lg_u32 %[cnt], 0\n\t" \
    "s_cbranch_scc1 1b\n\t"
#define AT_C10(a) "v" #a "0", "v" #a "1", "v" #a "2", "v" #a "3", "v" #a "4", "v" #a "5", "v" #a "6", "v" #a "7", "v" #a "8", "v" #a "9"
#define AT_CLOB "v192", "v193", "v194", "v195", "v196", "v197", "v198", "v199", AT_C10(20), AT_C10(21), AT_C10(22), AT_C10(23), AT_C10(24), "v250", "v251", "v252", "v253", "v254", "v255"

__global__ void __launch_bounds__(512, 2)
fused_block_kernel(const char* __restrict__ x, const char* __restrict__ wq, const char* __restrict__ wk, const char* __restrict__ wv,
                   const char* __restrict__ wo, const char* __restrict__ w1, const char* __restrict__ w2, const char* __restrict__ w3,
                   const char* __restrict__ w4, char* __restrict__ out, char* __restrict__ ws) {
    extern __shared__ char smem[];
    const unsigned sm = (unsigned)(uintptr_t)(LDSP char*)smem;
    const int b = blockIdx.x, tid = threadIdx.x, lane = tid & 63;
    const int w = __builtin_amdgcn_readfirstlane(tid >> 6);
#define PRIO 1
#define DRY 0
#define APOLL 1
#define PLATE 1
#define PSLP 0
#define P2F 0
#define PROJ2 1
#define EARLYG 0
#define BARPOS 0
#define GEL2 0
#define CSMOVE 1
#define HDLY 0
#define XDLY 13
#define ODLY 0
#define HHDLY 0
#define GDLY 0
#define G2DLY 0
#define HHORD 0
#define KDLY 0
#define LWS 1
#define FASTC 0
#define SCB 1
#define OUTPF 0
#define KVR 8
#define KVS 0x1100
#define KVX 0
#define W3AUX 16
#define W4AUX 16
constexpr unsigned KVB = KVR > 1 ? 0x8000u : OFF_KVF;
#define BRING 1
#define CSSH 0
#define HBORD 0
#define HBD 20
#define HDA 8
#define HDB 12
#define XSEL 0
#define LDLY 0
#define AW3 0
#define W3ORD 2
#define WSL 4
#define SIGC 1
#define PSL 1
#define PACE 0
#define KVD 2
#define PFSET 0
#define JROT 0
#define PF2 3
#define CPF 1
#define KARG 1
#define WAUX 16
#define SBARS 0
#define GDEPTH 2
#define GDP 1
#define OAUX 0
    unsigned cpf;
    {
        u64 pc; unsigned len;
        asm volatile("s_getpc_b64 %0\n.Lfbk_here:\n\ts_mov_b32 %1, .Lfbk_code_end-.Lfbk_here" : "=s"(pc), "=s"(len));
        unsigned o = ((b >> 3) * CPFL + (lane & (CPFL - 1))) * 128;
        len += CPFX;
        o = o < len ? o : len;
        asm volatile("global_load_dword %0, %1, %2" : "=v"(cpf) : "v"(o), "s"(pc) : "memory");
    }
    asm volatile("" :: "s"(x), "s"(wq), "s"(wk), "s"(wv), "s"(wo), "s"(w1), "s"(w2), "s"(w3), "s"(w4), "s"(out), "s"(ws));
    const int grp = (b & 7) * 4 + (b >> 6), j = ((b >> 3) + JROT) & 7;
    const int T0 = grp * 64;
    int dead = 0;
    STAMP_DECL;
    STAMP(0); CSTAMP(62);
    const rsrc_t rws = mkrsrc(ws, WS_USED);

    constexpr unsigned XT = R0, WQ = R1, WK = R2, WV = R2 + 16384;
    constexpr unsigned LF = R2 + 32768 - 128;
    u64 eraw;
    asm volatile("global_load_dwordx2 %0, %1, %2 sc1" : "=v"(eraw) : "v"(OFF_E), "s"(ws) : "memory");
    const int tq = w & 3, kh = w >> 2, g4 = lane >> 4, t16 = lane & 15;
    const int tokq = T0 + 16 * tq + t16;
    auto dma_x4 = [&](int row0) {
        const unsigned l16 = lane * 16, c = (row0 & 15) * 16;
        dma16x4(x + (size_t)(T0 + row0) * 1024, l16 ^ c, l16 ^ (c + 16), l16 ^ (c + 32), l16 ^ (c + 48), sm + XT + row0 * 1024);
    };
    const int u = w - 2;
    auto xrow_of = [&](int r) { return r < 8 * j ? r : r + 8; };
    if (w < 2) {
        dma_x4(8 * j); dma_x4(8 * j + 4);
        const int rp = lane >> 1, r = rp ^ (((rp >> 3) & 1) << 2);
        const unsigned voff = r * 32 + (lane & 1) * 16;
#pragma unroll
        for (int i = 0; i < 4; i++) dma16x4((w == 0 ? wk : wv) + i * 4096, voff, voff, voff, voff, sm + (w == 0 ? WK : WV) + i * 4096);
    } else {
        const int rr = lane >> 3, c = lane & 7;
        const unsigned voff = rr * 1024 + ((((c >> 1) ^ swz128(rr)) << 1) | (c & 1)) * 16;
        auto dma_q4 = [&](int g) { dma16x4(wq + (size_t)g * 32768 + j * 128, voff, voff + 7168, voff + 14336, voff + 21504, sm + WQ + g * 4096); };
        if (LDLY) __builtin_amdgcn_s_sleep(LDLY);
        dma_x4(xrow_of(4 * u)); dma_x4(xrow_of(4 * u + 24));
        if (u < 2) dma_x4(xrow_of(4 * u + 48)); else dma_q4(u - 2);
        dma_q4(u + 4); dma_q4(u + 10);
    }
    unsigned pfd;
    {
        const int m = w == 2 ? 0 : (w == 3 ? 1 : 2);
        const int sub = lane >> 4;
        const char* base = m == 0 ? w3 : (m == 1 ? w4 : (sub == 0 ? wo : (sub == 1 ? w1 : w2)));
        const unsigned n = m == 2 ? 16u : 56u;
        const unsigned l = w > 5 ? 0 : (m == 2 ? (lane & 15) : (lane < 56 ? lane : 55));
        const unsigned c3 = (3u - ((unsigned)(uintptr_t)base >> 7)) & 7u;
        const unsigned line = w != 5 ? b * n + l : ((b >> 3) * n + l) * 8 + c3;
        asm volatile("s_cmp_lt_u32 %2, 2\n\ts_cbranch_scc1 1f\n\tglobal_load_dword %0, %1, off\n1:" : "=v"(pfd) : "v"(base + line * 128), "s"(w) : "memory", "scc");
    }
    STAMP(17);
    const int nh = w & 1, th = (w >> 1) & 1;
    const int t32 = lane & 31, hi = lane >> 5;
    unsigned qa0, qa1, qxs[8];
    {
        const int xrow = 32 * th + t32;
        const int q = t16 >> 2, p = t16 & 3, cb = 2 * nh + ((lane >> 4) & 1);
        const int r0 = 8 * hi + q + (w < 4 ? 256 * th : 0);
        qa0 = opq(sm + WQ + r0 * 128 + ((cb ^ swz128(r0)) * 32) + p * 8);
        qa1 = opq(sm + WQ + (r0 + 4) * 128 + ((cb ^ swz128(r0 + 4)) * 32) + p * 8);
#pragma unroll
        for (int i = 0; i < 8; i++) qxs[i] = opq(sm + XT + xrow * 1024 + (((2 * i + hi) ^ (xrow & 15)) * 16));
    }
    f32x2 rst = {0.f, 0.f};
    int rsta = -1;
    u64 epoch;
    auto get_epoch = [&]() {
        unsigned lo, hi_;
        asm volatile("v_readfirstlane_b32 %0, %2\n\tv_readfirstlane_b32 %1, %3" : "=s"(lo), "=s"(hi_) : "v"((unsigned)eraw), "v"((unsigned)(eraw >> 32)));
        epoch = (((u64)hi_ << 32) | lo) + 1;
        asm volatile("" :: "v"(cpf));
    };
    const u32x4 ones4 = {0x3F803F80u, 0x3F803F80u, 0x3F803F80u, 0x3F803F80u};
    if (w < 2) {
        const int xrow = 8 * j + (t16 & 7);
        const unsigned xa = sm + XT + xrow * 1024;
        const int q = t16 >> 2, p = t16 & 3;
        const int r0 = 8 * g4 + q, r1 = r0 + 4;
        const unsigned wbase = sm + (w == 0 ? WK : WV);
        const unsigned a0 = opq(wbase + (r0 ^ (((r0 >> 3) & 1) << 2)) * 32 + p * 8);
        const unsigned a1 = opq(wbase + (r1 ^ (((r1 >> 3) & 1) << 2)) * 32 + p * 8);
        unsigned xs[4];
#pragma unroll
        for (int i = 0; i < 4; i++) xs[i] = opq(xa + (((4 * i + g4) ^ (xrow & 15)) * 16));
        waitvm<0>();
        get_epoch();
        STAMP(18);
        f32x4 acc = {}, gm = {};
        const bool tokrow = t16 < 8;
        auto proj = [&](auto wfirst) {
            constexpr int DP = 4;
            u32x4 xf[DP + 1]; bf16x8 wf[DP + 1];
#pragma unroll
            for (int d = 0; d < DP; d++) { xf[d] = ldsr128(xs[d & 3] + (d >> 2) * 256); wf[d] = trfrag(a0 + d * 1024, a1 + d * 1024); }
#pragma unroll
            for (int s = 0; s < 16; s++) {
                SBAR();
                if (s + DP < 16) { const int n = s + DP; xf[n % (DP + 1)] = ldsr128(xs[n & 3] + (n >> 2) * 256); wf[n % (DP + 1)] = trfrag(a0 + n * 1024, a1 + n * 1024); }
                SBAR();
                u32x4 xr = xf[s % (DP + 1)];
#pragma unroll
                for (int i = 0; i < 4; i++) xr[i] = tokrow ? xr[i] : 0x3F803F80u;
                const bf16x8 xv = asbf(xr), wv_ = wf[s % (DP + 1)];
                acc = wfirst.value ? mfma16(wv_, xv, acc) : mfma16(xv, wv_, acc);
                gm = mfma16(xv, xv, gm);
            }
            SBAR();
        };
        if (w == 0) proj(BoolC<true>{}); else proj(BoolC<false>{});
        STAMP(1);
        if (w == 0) {
            const int r3 = t16 & 3;
            float sq = r3 == 0 ? gm[0] : (r3 == 1 ? gm[1] : (r3 == 2 ? gm[2] : gm[3]));
            sq = (t16 >> 2) == g4 ? sq : 0.f;
            sq = x32sum(x16sum(sq));
            const float su = x32sum(x16sum(g4 == 2 ? gm[0] : 0.f));
            const float mu = su * (1.f / 512.f);
            const float rs = __builtin_amdgcn_rsqf(fmaxf(sq * (1.f / 512.f) - mu * mu, 0.f) + 1e-5f);
            rst = f32x2{mu, rs};
            rsta = lane < 8 ? (8 * j + lane) * 8 : -1;
#pragma unroll
            for (int i = 0; i < 4; i++) acc[i] = (acc[i] - mu * dppf<0x128, 0xf>(acc[i])) * rs;
        } else {
#pragma unroll
            for (int i = 0; i < 4; i++) {
                const float sq = bitsf(__builtin_amdgcn_ds_bpermute((20 * g4 + i) * 4, fbits(gm[i])));
                const float su = bitsf(__builtin_amdgcn_ds_bpermute((16 * g4 + 8) * 4, fbits(gm[i])));
                const auto cx = __builtin_amdgcn_permlane32_swap(fbits(acc[i]), fbits(acc[i]), false, false);
                const float mu = su * (1.f / 512.f);
                const float rs = __builtin_amdgcn_rsqf(fmaxf(sq * (1.f / 512.f) - mu * mu, 0.f) + 1e-5f);
                acc[i] = (acc[i] - mu * bitsf(cx[1])) * rs;
            }
        }
        if (w == 0) {
            const unsigned k01 = pk2(acc[0], acc[1]), k23 = pk2(acc[2], acc[3]);
            auto s0 = __builtin_amdgcn_permlane32_swap(k01, k01, false, false);
            auto s1 = __builtin_amdgcn_permlane32_swap(k23, k23, false, false);
            u32x4 kv = {s0[0], s1[0], s0[1], s1[1]};
            const int tile = 2 * grp + (j >> 2), kk = 8 * (j & 3) + t16;
            if (lane < 32 && t16 < 8) st_sc1(kv, rws, tile * 1024 + g4 * 512 + kk * 16, OFF_KI);
            float n2 = dot2(k01, k01, dot2(k23, k23, 0.f));
            n2 = x32sum(x16sum(n2));
            n2 = t16 < 8 ? n2 : 0.f;
            const float kmax2 = wave_max_nn(n2);
            const unsigned pay = (fbits(kmax2) + 0xFFFFu) >> 16;
            STAMP(50);
            waitvm<0>();
            STAMP(51);
            if (lane < KVR) flag_store(ws, KVB + lane * KVS + (grp * 8 + j) * 16, (epoch << 16) | (u64)(pay & 0xFFFFu));
        } else {
            const unsigned v01 = pk2(acc[0], acc[1]), v23 = pk2(acc[2], acc[3]);
            auto s0 = __builtin_amdgcn_permlane16_swap(v01, v01, false, false);
            auto s1 = __builtin_amdgcn_permlane16_swap(v23, v23, false, false);
            u32x4 vv = {s0[0], s1[0], s0[1], s1[1]};
            if (lane == 16) vv = u32x4{0x3F803F80u, 0x3F803F80u, 0x3F803F80u, 0x3F803F80u};
            const int tile = 2 * grp + (j >> 2), blk = j & 3;
            if (lane < 17) st_sc1(vv, rws, tile * 1536 + blk * 384 + lane * 16, OFF_VI);
            float am = fmaxf(fmaxf(fabsf(bflo(v01)), fabsf(bfhi(v01))), fmaxf(fabsf(bflo(v23)), fabsf(bfhi(v23))));
            am = lane < 32 ? am : 0.f;
            const float vmax = wave_max_nn(am);
            const unsigned pay = (fbits(vmax) + 0xFFFFu) >> 16;
            waitvm<0>();
            if (lane < KVR) flag_store(ws, KVB + lane * KVS + (grp * 8 + j) * 16 + 8, (epoch << 16) | (u64)(pay & 0xFFFFu));
        }
    } else {
        const int nrow = u < 2 ? 12 : 8;
        const int tn = t16 < nrow ? t16 : (t16 & 7);
        const int row = xrow_of(4 * u + 24 * (tn >> 2) + (tn & 3));
        unsigned sa[4];
#pragma unroll
        for (int i = 0; i < 4; i++) sa[i] = opq(sm + XT + row * 1024 + (((4 * i + g4) ^ (row & 15)) * 16));
        waitvm<9>();
        get_epoch();
        STAMP(25);
        f32x4 su = {}, gm = {};
        u32x4 xf[16];
#pragma unroll
        for (int s = 0; s < 16; s++) xf[s] = ldsr128(sa[s & 3] + (s >> 2) * 256);
#pragma unroll
        for (int s = 0; s < 16; s++) { su = mfma16(asbf(ones4), asbf(xf[s]), su); gm = mfma16(asbf(xf[s]), asbf(xf[s]), gm); }
        const int r3 = t16 & 3;
        const float sq = r3 == 0 ? gm[0] : (r3 == 1 ? gm[1] : (r3 == 2 ? gm[2] : gm[3]));
        const float mu = su[0] * (1.f / 512.f);
        const float var = fmaxf(sq * (1.f / 512.f) - mu * mu, 0.f);
        rst = f32x2{mu, __builtin_amdgcn_rsqf(var + 1e-5f)};
        rsta = (t16 >> 2) == g4 && t16 < nrow ? row * 8 : -1;
        waitvm<1>();
    }
    bar();
    STAMP(2);
    constexpr unsigned SC = R2;
    constexpr unsigned QB = SC + 4096;
    const int kq = w & 3;
    const unsigned kvfo = KVB + ((KVX ? b >> 3 : b) & (KVR - 1)) * KVS + (64 * kq + lane) * 16;
    u32x4 kvf;
    f32x16 qacc = {};
    {
        if (tid < 32) *(LDSP unsigned*)(sm + LF + tid * 4) = 0u;
        if (rsta >= 0) *(LDSP f32x2*)(sm + SC + rsta) = rst;
        if (w < 4) {
            const unsigned sv = ((g4 ^ t16) & 1) ? 0u : 0x3F803F80u;
            const u32x4 sel = {sv, sv, sv, sv};
            f32x4 cs = {};
            bf16x8 wf[16];
#pragma unroll
            for (int s = 0; s < 16; s++) wf[s] = trfrag(qa0 + s * 2048, qa1 + s * 2048);
#pragma unroll
            for (int s = 0; s < 16; s++) cs = mfma16(wf[s], asbf(sel), cs);
            if (t16 < 2) ldswf4(sm + SC + 512 + (th * 64 + 32 * nh + 16 * t16 + 4 * g4) * 4, cs);
        } else {
            constexpr int DP = 4;
            u32x4 xf[DP + 1]; bf16x8 wf[DP + 1];
#pragma unroll
            for (int d = 0; d < DP; d++) { xf[d] = ldsr128(qxs[d & 7] + (d >> 3) * 256); wf[d] = trfrag(qa0 + d * 2048, qa1 + d * 2048); }
#pragma unroll
            for (int s = 0; s < 32; s++) {
                SBAR();
                if (s + DP < 32) { const int n = s + DP; xf[n % (DP + 1)] = ldsr128(qxs[n & 7] + (n >> 3) * 256); wf[n % (DP + 1)] = trfrag(qa0 + n * 2048, qa1 + n * 2048); }
                SBAR();
                qacc = mfma32(wf[s % (DP + 1)], asbf(xf[s % (DP + 1)]), qacc);
            }
            SBAR();
        }
        STAMP(19);
        if (!APOLL || !PLATE) kvf = ld_sc1(rws, kvfo, 0);
        bar();
        if (APOLL && PLATE && w < 4) { if (KDLY) __builtin_amdgcn_s_sleep(KDLY); kvf = ld_sc1(rws, kvfo, 0); }
        STAMP(20);
        if (w >= 4) {
            const int xrow = 32 * th + t32;
            const f32x2 st = *(LDSP f32x2*)(sm + SC + xrow * 8);
            f32x4 c[4];
#pragma unroll
            for (int i = 0; i < 4; i++) {
                const unsigned ca = sm + SC + 512 + (32 * nh + 8 * i + 4 * hi) * 4;
                c[i] = ldsrf4(ca) + ldsrf4(ca + 256);
            }
            const float mu = st[0];
            const float sc = st[1] * (0.25f * 1.4426950408889634f);
            float qv[16];
#pragma unroll
            for (int i = 0; i < 16; i++) qv[i] = (qacc[i] - mu * c[i >> 2][i & 3]) * sc;
#pragma unroll
            for (int h = 0; h < 2; h++)
                ldsw128(sm + QB + (th * 4 + 2 * nh + h) * 1024 + lane * 16,
                        u32x4{pk2(qv[8 * h], qv[8 * h + 1]), pk2(qv[8 * h + 2], qv[8 * h + 3]), pk2(qv[8 * h + 4], qv[8 * h + 5]), pk2(qv[8 * h + 6], qv[8 * h + 7])});
        }
    }
    f32x16 oacc[4];
    float mrow[4];
#pragma unroll
    for (int i = 0; i < 4; i++) mrow[i] = 0.f;
    const unsigned koff = OFF_KI + kq * 16 * 1024, voffb = OFF_VI + kq * 16 * 1536;
    const int vr = lane & 31;
    const unsigned kl = opq(lane * 16), vl = opq(vr < 16 ? (lane >> 5) * 384 + vr * 16 : ((vr & 11) == 0 ? (lane >> 5) * 384 + 256 : 0x80000000u));
    const unsigned fo = opq((g4 * 2048 + tokq) * 16);
    const bool s4 = (w & 2) != 0;
    const char* praw;
    unsigned vraw[4];
    {
        const char* p3 = w3 + (size_t)(32 * (grp >> 1)) * 3584 + 448 * j + 32 * 7 * (grp & 1) - 3072;
        const char* p4 = w4 + (size_t)(224 * (grp >> 2)) * 1024 + 128 * j + 32 * (grp & 3) - 3072;
        asm volatile("" : "+s"(p3), "+s"(p4));
        praw = s4 ? p4 : p3;
        const unsigned fstep = s4 ? 32768u : 32u, vr0 = (lane >> 1) * (s4 ? 1024u : 3584u) + (lane & 1) * 16;
#pragma unroll
        for (int k = 0; k < 4; k++) { const int f = 4 * (w & 1) + k; vraw[k] = opq(vr0 + (f < 7 ? f : 6) * fstep + 3072 - 1024 * k); }
        asm volatile("" : "+s"(praw));
    }
    const unsigned rawl = sm + R0 + (w & 3) * 16384;
    const unsigned vo = opq((lane >> 3) * 1024 + ((((lane & 7) >> 1) ^ swz128(lane >> 3)) << 1 | (lane & 1)) * 16);
    const char* pwo = wo + (size_t)(128 * (w & 3)) * 1024 + j * 128;
    const char* pw1 = w1 + (size_t)(128 * (w & 3)) * 1024 + j * 128;
    asm volatile("" : "+s"(pwo), "+s"(pw1));
    STAMP(52);
    u32x4 kf[4], v0[4], v1[4];
    auto ring = [&]() {
#pragma unroll
        for (int t = 0; t < 3; t++) {
            kf[t] = ld_sc1(rws, kl, koff + t * 1024);
            v0[t] = ld_sc1(rws, vl, voffb + t * 1536);
            v1[t] = ld_sc1(rws, vl, voffb + t * 1536 + 768);
            SBAR();
        }
    };
    float kmax2;
    u64 vbad;
    if (!APOLL || w < 4) {
        const unsigned tg0 = (unsigned)(epoch & 0xFFFFu) << 16, tg1 = (unsigned)(epoch >> 16);
        int it = dead ? SPIN_MAX : 0;
        auto okf = [&](const u32x4& f) { return __all((f[0] & 0xFFFF0000u) == tg0 && f[1] == tg1 && (f[2] & 0xFFFF0000u) == tg0 && f[3] == tg1); };
        if (P2F) {
            u32x4 kv2;
#pragma unroll 1
            for (; it < SPIN_MAX; it++) {
                kv2 = ld_sc1(rws, kvfo, 0);
                SBAR();
                if (okf(kvf)) break;
                asm volatile("" ::: "memory");
                kvf = ld_sc1(rws, kvfo, 0);
                SBAR();
                if (okf(kv2)) { kvf = kv2; break; }
                asm volatile("" ::: "memory");
            }
        } else {
#pragma unroll 1
            for (; it < SPIN_MAX; it++) {
                if (okf(kvf)) break;
                if (PSLP) __builtin_amdgcn_s_sleep(PSLP);
                asm volatile("" ::: "memory");
                kvf = ld_sc1(rws, kvfo, 0);
            }
        }
        asm volatile("" ::: "memory");
        if (it == SPIN_MAX) dead = 1;
        asm volatile("" :: "v"(pfd));
        ring();
        kmax2 = wave_max_nn(bitsf(kvf[0] << 16));
        vbad = __builtin_amdgcn_ballot_w64(!(bitsf(kvf[2] << 16) <= 1.0e12f));
        if (APOLL && lane == 0) *(LDSP f32x2*)(sm + SC + 1024 + kq * 8) = f32x2{kmax2, vbad ? 1.f : 0.f};
    } else {
        waitvm<0>();
        asm volatile("" :: "v"(pfd));
    }
    v1[3] = u32x4{0, 0, 0, 0};
    asm volatile("" : "+v"(v1[3]));
    SBAR();
    STAMP(4);
    bar();
    STAMP(21);
    if (APOLL && w >= 4) {
        if (!BRING) ring();
        const f32x2 kb = *(LDSP f32x2*)(sm + SC + 1024 + kq * 8);
        kmax2 = bitsf(__builtin_amdgcn_readfirstlane(fbits(kb[0])));
        vbad = __builtin_amdgcn_readfirstlane(fbits(kb[1]));
    }
    asm volatile("" : "+s"(kmax2), "+s"(vbad));
    bf16x8 qf[4];
#pragma unroll
    for (int i = 0; i < 4; i++) qf[i] = asbf(ldsr128(sm + QB + ((w >> 2) * 4 + i) * 1024 + lane * 16));
    bar();
    if (APOLL && BRING && w >= 4) ring();
    STAMP(3);
    constexpr unsigned PL = 4 * 2048 * 16;
    u32x4 e4 = {EMPTY, EMPTY, EMPTY, EMPTY};
    asm volatile("" : "+v"(e4));
    {
        CSTAMP(56);
        __builtin_amdgcn_s_setprio(1);
        float qn = 0.f;
#pragma unroll
        for (int i = 0; i < 4; i++) {
            u32x4 qv = __builtin_bit_cast(u32x4, qf[i]);
            float t = dot2(qv[0], qv[0], dot2(qv[1], qv[1], dot2(qv[2], qv[2], dot2(qv[3], qv[3], 0.f))));
            qn = fmaxf(qn, x32sum(t));
        }
        const bool fast = (vbad | __builtin_amdgcn_ballot_w64(!(qn * kmax2 <= 3600.f))) == 0;
        const f32x16 zero = {};
        if (__builtin_expect(fast, 1)) {
            const u64 wsa = (u64)(uintptr_t)ws;
            const u32x4 rs = {(unsigned)wsa, (unsigned)(wsa >> 32) & 0xFFFFu, WS_USED, 0x00020000u};
            unsigned sk = koff + 3 * 1024, sv = voffb + 3 * 1536, sw = voffb + 3 * 1536 + 3072;
            unsigned st0 = OFF_O + 2 * j * PL, st1 = st0 + PL, st2 = OFF_G + 7 * j * PL, st3 = st2 + PL;
            unsigned kl2, vl2, fo2, cnt, msk, m0s, mb, ma = sm + R1 + (w & 3) * 16384;
            asm volatile(
                "s_nop 1\n\t"
                "v_mfma_f32_32x32x16_bf16 v[192:207], %[k0], %[q0], 0\n\t"
                "v_mfma_f32_32x32x16_bf16 %[o3], %[v13], %[v13], 0\n\t"
                "v_mfma_f32_32x32x16_bf16 %[o0], %[v13], %[v13], 0\n\t"
                "v_mfma_f32_32x32x16_bf16 %[o1], %[v13], %[v13], 0\n\t"
                "v_mfma_f32_32x32x16_bf16 %[o2], %[v13], %[v13], 0\n\t"
                "v_mov_b32 v248, 0\n\tv_mov_b32 v249, 0\n\tv_mov_b32 v250, 0\n\tv_mov_b32 v251, 0\n\t"
                "v_mov_b32 v252, 0\n\tv_mov_b32 v253, 0\n\tv_mov_b32 v254, 0\n\tv_mov_b32 v255, 0\n\t"
                "s_cmp_lt_u32 %[w], 4\n\t"
                "s_cbranch_scc0 2f\n\t"
                "s_mov_b32 %[m0s], m0\n\t"
                AT_DMA(rawl, "%[praw]")
                "v_mov_b32 %[r0], %[vo]\n\tv_add_u32 %[r1], 7168, %[vo]\n\tv_add_u32 %[r2], 14336, %[vo]\n\tv_add_u32 %[r3], 21504, %[vo]\n\t"
                "s_mov_b64 s[88:89], %[pw1]\n\t"
                AT_LOOP(AT_ST, "s_waitcnt vmcnt(13)\n\t", "s_waitcnt vmcnt(17)\n\t", AT_DMA(ma, "s[88:89]"), AT_DMA(mb, "s[90:91]"),
                        "s_cmp_eq_u32 %[cnt], 2\n\ts_cselect_b64 s[88:89], %[pwo], s[88:89]\n\ts_cselect_b32 %[ma], %[mwo], %[ma]\n\t"
                        "s_cmp_eq_u32 %[cnt], 1\n\ts_cselect_b32 %[mb], 0, 0x8000\n\ts_cselect_b32 %[msk], 0x80000000, 0\n\tv_or_b32 %[fo2], %[msk], %[fo]\n\t"
                        "s_add_u32 s90, s88, %[mb]\n\ts_addc_u32 s91, s89, 0\n\ts_lshr_b32 %[mb], %[mb], 3\n\ts_add_u32 %[mb], %[mb], %[ma]\n\t",
                        "s_add_u32 %[st0], %[st0], 0x200000\n\ts_add_u32 %[st1], %[st1], 0x200000\n\ts_add_u32 %[st2], %[st2], 0x40000\n\ts_add_u32 %[st3], %[st3], 0x40000\n\t"
                        "s_add_u32 s88, s88, 0x10000\n\ts_addc_u32 s89, s89, 0\n\ts_add_u32 %[ma], %[ma], 8192\n\t")
                "s_mov_b32 m0, %[m0s]\n\t"
                "s_waitcnt vmcnt(5)\n\t"
                "s_branch 3f\n"
                "2:\n\t"
                AT_LOOP(AT_NOST, "s_waitcnt vmcnt(6)\n\t", "s_waitcnt vmcnt(6)\n\t", "", "",
                        "s_cmp_eq_u32 %[cnt], %[sigc]\n\ts_cbranch_scc0 4f\n\tds_write_b32 %[sig], %[idx]\n4:\n\t", "")
                "s_waitcnt vmcnt(0)\n"
                "3:\n\t"
                "v_smfmac_f32_32x32x32_bf16 %[o3], %[v13], v[248:255], %[idx]\n\t"
                "s_nop 7\n\ts_nop 7\n\ts_nop 3"
                : [o0] "=&{v[64:79]}"(oacc[0]), [o1] "=&{v[80:95]}"(oacc[1]), [o2] "=&{v[96:111]}"(oacc[2]), [o3] "=&{v[112:127]}"(oacc[3]),
                  [k0] "+{v[128:131]}"(kf[0]), [k1] "+{v[132:135]}"(kf[1]), [k2] "+{v[136:139]}"(kf[2]), [k3] "=&{v[140:143]}"(kf[3]),
                  [v00] "+{v[144:147]}"(v0[0]), [v01] "+{v[148:151]}"(v0[1]), [v02] "+{v[152:155]}"(v0[2]), [v03] "=&{v[156:159]}"(v0[3]),
                  [v10] "+{v[160:163]}"(v1[0]), [v11] "+{v[164:167]}"(v1[1]), [v12] "+{v[168:171]}"(v1[2]), [v13] "+{v[172:175]}"(v1[3]),
                  [kl2] "=&v"(kl2), [vl2] "=&v"(vl2), [fo2] "=&v"(fo2),
                  [sk] "+s"(sk), [sv] "+s"(sv), [sw] "+s"(sw), [st0] "+s"(st0), [st1] "+s"(st1), [st2] "+s"(st2), [st3] "+s"(st3),
                  [cnt] "=&s"(cnt), [msk] "=&s"(msk), [m0s] "=&s"(m0s), [ma] "+s"(ma), [mb] "=&s"(mb),
                  [r0] "+v"(vraw[0]), [r1] "+v"(vraw[1]), [r2] "+v"(vraw[2]), [r3] "+v"(vraw[3])
                : [q0] "{v[176:179]}"(qf[0]), [q1] "{v[180:183]}"(qf[1]), [q2] "{v[184:187]}"(qf[2]), [q3] "{v[188:191]}"(qf[3]), [idx] "v"(0xDDDDDDDDu), [kl] "v"(kl), [vl] "v"(vl),
                  [fo] "v"(fo), [e4] "v"(e4), [rs] "s"(rs), [w] "s"(w),
                  [praw] "s"(praw), [rawl] "s"(rawl), [vo] "v"(vo), [sig] "v"(sm + LF + 64 + kq * 4), [sigc] "n"(SIGC), [pw1] "s"(pw1), [pwo] "s"(pwo + 32768), [mwo] "s"(rawl + 4096)
                : "memory", "scc", "s88", "s89", "s90", "s91", AT_CLOB);
        } else {
            if (w < 4) {
                dma16x4(praw, vraw[0], vraw[1], vraw[2], vraw[3], rawl);
#pragma unroll 1
                for (int i = 1; i < 8; i++)
                    dma16x4((i < 4 ? pwo : pw1) + (i & 3) * 32768, vo, vo + 7168, vo + 14336, vo + 21504, sm + (i < 4 ? R0 : R1) + w * 16384 + (i & 3) * 4096);
#pragma unroll 1
                for (int i = 0; i < 15; i++) st_sc1(e4, rws, fo, i < 8 ? OFF_O + (i >> 1) * 0x200000 + (2 * j + (i & 1)) * PL : OFF_G + (7 * j + i - 8) * PL);
            }
#pragma unroll
            for (int i = 0; i < 4; i++) { mrow[i] = -3.0e38f; oacc[i] = f32x16{}; }
#pragma unroll 1
            for (int t = 0; t < 16; t++) {
                u32x4 kx = ld_sc1(rws, kl, koff + t * 1024);
                u32x4 va = ld_sc1(rws, vl, voffb + t * 1536);
                u32x4 vb = ld_sc1(rws, vl, voffb + t * 1536 + 768);
#pragma unroll
                for (int i = 0; i < 4; i++) {
                    f32x16 s = mfma32(asbf(kx), qf[i], zero);
                    float mx = s[0];
#pragma unroll
                    for (int e = 1; e < 16; e++) mx = fmaxf(mx, s[e]);
                    mx = x32max(mx);
                    const float mn = fmaxf(mrow[i], mx);
                    const float al = __builtin_amdgcn_exp2f(mrow[i] - mn);
                    mrow[i] = mn;
#pragma unroll
                    for (int e = 0; e < 16; e++) { s[e] = __builtin_amdgcn_exp2f(s[e] - mn); oacc[i][e] *= al; }
                    oacc[i] = pvmac(asbf(va), s, 0, oacc[i]);
                    oacc[i] = pvmac(asbf(vb), s, 1, oacc[i]);
                }
            }
            if (w >= 4) lds_signal(sm + LF + 64 + kq * 4, lane);
        }
    }
    STAMP(5); CSTAMP(57);
    {
    const int lane = opq(tid) & 63, g4 = lane >> 4, t16 = lane & 15, tokq = T0 + 16 * tq + t16;
    const unsigned fo = (g4 * 2048 + tokq) * 16;
    u32x4 w3f[32];
    const unsigned w3o = w < 7 ? j * WT_J + w * 1024 + lane * 16 : 0x80000000u;
    auto ldw3 = [&](int i) { w3f[i] = ld_aux<W3AUX>(rws, w3o, OFF_W3T + i * 7168); };
    auto ldw3b = [&](int i) { if (!AW3 || w >= 4) ldw3(i); };
    {
        const int th = w >> 2, kq = w & 3;
        constexpr unsigned RS = R2;
        if (w < 4) {
            st_sc1_64(u32x2{e4[0], e4[1]}, rws, ((2 * j + (g4 & 1)) * 2048 + tokq) * 8 + (g4 >> 1) * (OFF_XS - OFF_HS), OFF_HS);
            waitvm<1>();
            const unsigned ta = sm + R0 + w * 16384 + (4 * (lane >> 4) + ((lane & 15) >> 2)) * 32 + (lane & 3) * 8;
            u32x4 fr[4];
#pragma unroll
            for (int i = 0; i < 4; i++) fr[i] = __builtin_bit_cast(u32x4, trfrag(ta + i * 1024, ta + i * 1024 + 512));
#pragma unroll
            for (int i = 0; i < 4; i++) {
                const int f0 = 4 * (w & 1) + i, f = f0 < 7 ? f0 : 6;
                const int c = 7 * (grp & 1) + f;
                unsigned d3 = OFF_W3T + (((grp >> 1) * 2 + (c & 1)) * 7 + (c >> 1)) * 1024, d4 = OFF_W4T + ((f * 4 + (grp & 3)) * 8 + (grp >> 2)) * 1024;
                asm volatile("" : "+s"(d3), "+s"(d4));
                st_sc1(fr[i], rws, lane * 16, ((w & 2) ? d4 : d3) + j * WT_J);
            }
            STAMP(54);
            asm volatile("s_waitcnt lgkmcnt(0)" ::: "memory");
            const unsigned vo = (lane >> 3) * 1024 + ((((lane & 7) >> 1) ^ swz128(lane >> 3)) << 1 | (lane & 1)) * 16;
            dma16x4(wo + (size_t)(128 * w) * 1024 + j * 128, vo, vo + 7168, vo + 14336, vo + 21504, sm + R0 + w * 16384);
            STAMP(55);
        }
        if (th == 1) lds_wait8<LWS>(sm + LF + 32, dead);
#pragma unroll
        for (int hd = 0; hd < 4; hd++) {
            if (hd != kq) {
                const unsigned a = sm + RS + (hd * 3 + (((kq - hd) & 3) - 1)) * 2560;
                ldswf4(a + lane * 16, f32x4{oacc[hd][0], oacc[hd][1], oacc[hd][2], oacc[hd][3]});
                ldswf4(a + 1024 + lane * 16, f32x4{oacc[hd][4], oacc[hd][5], oacc[hd][6], oacc[hd][7]});
                *(LDSP f32x2*)(a + 2048 + lane * 8) = f32x2{oacc[hd][8], mrow[hd]};
            }
        }
        lds_signal(sm + LF + th * 16 + kq * 4, lane);
        f32x4 oa, ob; float l, m;
        if (kq == 0) { oa = f32x4{oacc[0][0], oacc[0][1], oacc[0][2], oacc[0][3]}; ob = f32x4{oacc[0][4], oacc[0][5], oacc[0][6], oacc[0][7]}; l = oacc[0][8]; m = mrow[0]; }
        else if (kq == 1) { oa = f32x4{oacc[1][0], oacc[1][1], oacc[1][2], oacc[1][3]}; ob = f32x4{oacc[1][4], oacc[1][5], oacc[1][6], oacc[1][7]}; l = oacc[1][8]; m = mrow[1]; }
        else if (kq == 2) { oa = f32x4{oacc[2][0], oacc[2][1], oacc[2][2], oacc[2][3]}; ob = f32x4{oacc[2][4], oacc[2][5], oacc[2][6], oacc[2][7]}; l = oacc[2][8]; m = mrow[2]; }
        else { oa = f32x4{oacc[3][0], oacc[3][1], oacc[3][2], oacc[3][3]}; ob = f32x4{oacc[3][4], oacc[3][5], oacc[3][6], oacc[3][7]}; l = oacc[3][8]; m = mrow[3]; }
        lds_wait4<LWS>(sm + LF + th * 16, dead);
        STAMP(22);
        f32x4 pa[3], pb[3]; f32x2 pl[3];
#pragma unroll
        for (int s = 0; s < 3; s++) {
            const unsigned a = sm + RS + (kq * 3 + s) * 2560;
            pa[s] = ldsrf4(a + lane * 16); pb[s] = ldsrf4(a + 1024 + lane * 16); pl[s] = *(LDSP f32x2*)(a + 2048 + lane * 8);
        }
        if (th == 0) lds_signal(sm + LF + 32 + kq * 4, lane);
        const float M = fmaxf(fmaxf(m, pl[0][1]), fmaxf(pl[1][1], pl[2][1]));
        const float f0 = __builtin_amdgcn_exp2f(m - M);
        oa *= f0; ob *= f0; l *= f0;
#pragma unroll
        for (int s = 0; s < 3; s++) {
            const float f = __builtin_amdgcn_exp2f(pl[s][1] - M);
            oa += pa[s] * f; ob += pb[s] * f; l = fmaf(pl[s][0], f, l);
        }
        const float il = 1.f / l;
        u32x4 ov = {san(pk2(oa[0] * il, oa[1] * il)), san(pk2(oa[2] * il, oa[3] * il)), san(pk2(ob[0] * il, ob[1] * il)), san(pk2(ob[2] * il, ob[3] * il))};
        if (w < 4) {
            waitvm<4>();
            STAMP(42);
            lds_signal(sm + LF + 48 + w * 4, lane);
            lds_wait4(sm + LF + 48, dead);
            STAMP(43);
            if (tid == 0) flag_store(ws, OFF_CLR + b * 8, epoch);
        }
        const int t32 = lane & 31, hi = lane >> 5;
        const int kstep = 2 * j + (kq >> 1), gp = 2 * (kq & 1) + hi;
        st_sc1(ov, rws, ((kstep * 4 + gp) * 2048 + T0 + 32 * th + t32) * 16, OFF_O);
        STAMP(23);
        if (th == 0) {
            __builtin_amdgcn_s_setprio(0);
            if (OUTPF) { const unsigned d_ = *(const volatile GASP unsigned*)(out + (size_t)(T0 + 16 * kq + (lane & 15)) * 1024 + j * 128); (void)d_; }
            lds_wait4<4>(sm + LF + 64, dead);
            STAMP(44);
            poll_flags(ws, OFF_CLR + (64 * kq + lane) * 8, true, epoch, dead);
            waitvm<0>();
            STAMP(45);
            if (AW3) {
                lds_signal(sm + LF + 80 + kq * 4, lane);
                lds_wait4(sm + LF + 80, dead);
#pragma unroll
                for (int i = 0; i < 32; i++) {
                    if (WSL && i && (i & 3) == 0) { SBAR(); __builtin_amdgcn_s_sleep(WSL); SBAR(); }
                    ldw3(i);
                }
                SBAR();
                STAMP(47);
            }
        }
        STAMP(24);
        bar();
        if (b == 0 && tid == 0) flag_store(ws, OFF_E, epoch);
    }
    STAMP(6); CSTAMP(59);
    __builtin_amdgcn_s_setprio(0);
    const int lq = t16 >> 2, lp = t16 & 3;
    constexpr unsigned XS0 = R2, XS1 = R2 + 16384;
    float hres[8], xres1[8];
    const int r0n = 4 * g4 + lq;
    unsigned wl[4];
#pragma unroll
    for (int nt = 0; nt < 4; nt++) wl[nt] = r0n * 128 + ((nt ^ swz128(r0n)) * 32) + lp * 8;
    auto gemm64 = [&](unsigned region, const u32x4 (&bf)[8], f32x4 (&acc)[4], auto side) {
        unsigned wa[4];
#pragma unroll
        for (int nt = 0; nt < 4; nt++) wa[nt] = opq(sm + region + 256 * kh * 128 + wl[nt]);
        if (GRING) gemm_r<8, 16 * 128, 4096, 4, GRING ? GRING : 1>(wa, bf, acc, side); else gemm_p<8, 16 * 128, 4096, 4, GDP>(wa, bf, acc, side);
    };
    auto xwrite = [&](unsigned xs, const f32x4 (&acc)[4]) {
        ldswf4(sm + xs + w * 2048 + lane * 16, kh ? acc[0] : acc[2]);
        ldswf4(sm + xs + w * 2048 + 1024 + lane * 16, kh ? acc[1] : acc[3]);
    };
    auto xread = [&](unsigned xs, const f32x4 (&acc)[4], f32x4& v0, f32x4& v1) {
        v0 = kh ? acc[2] : acc[0]; v1 = kh ? acc[3] : acc[1];
        v0 += ldsrf4(sm + xs + (w ^ 4) * 2048 + lane * 16);
        v1 += ldsrf4(sm + xs + (w ^ 4) * 2048 + 1024 + lane * 16);
    };
    const unsigned vo1024 = (lane >> 3) * 1024 + ((((lane & 7) >> 1) ^ swz128(lane >> 3)) << 1 | (lane & 1)) * 16;
    const unsigned vo3584 = (lane >> 3) * 3584 + ((((lane & 7) >> 1) ^ swz128(lane >> 3)) << 1 | (lane & 1)) * 16;
    auto dma_sq = [&](const char* wbase, int row0, unsigned region, int i) {
        dma16(wbase + (size_t)(row0 + 8 * i) * 1024 + j * 128, vo1024, sm + region + i * 1024);
    };
    auto dma_w3 = [&](int chunk, unsigned region, int i) {
        dma16(w3 + (size_t)(64 * w + 8 * i) * 3584 + chunk * 1024 + j * 128, vo3584, sm + region + w * 8192 + i * 1024);
    };
    auto nop_side = [](int) {};
    const unsigned xo = opq(tokq * 1024 + (64 * j + 32 * kh + 4 * g4) * 2);
    f32x4 cs1[4] = {};
    auto colsum1 = [&](auto side) {
        const u32x4 ones = {0x3F803F80u, 0x3F803F80u, 0x3F803F80u, 0x3F803F80u};
        u32x4 of[8];
#pragma unroll
        for (int s = 0; s < 8; s++) of[s] = ones;
        gemm64(R1, of, cs1, side);
    };
    f32x4 cs3[2] = {};
    auto colsum3 = [&](int s0, int s1) {
        const u32x4 ones = {0x3F803F80u, 0x3F803F80u, 0x3F803F80u, 0x3F803F80u};
#pragma unroll
        for (int s = s0; s < s1; s++) { cs3[0] = mfma16(asbf(w3f[2 * s]), asbf(ones), cs3[0]); cs3[1] = mfma16(asbf(w3f[2 * s + 1]), asbf(ones), cs3[1]); }
    };
    {
        u32x2 xres[2];
#pragma unroll
        for (int i = 0; i < 2; i++) xres[i] = *(const GASP u32x2*)(x + xo + 32 * i);
        const bool w3first = W3ORD == 0 || (W3ORD == 2 && w < 4) || (W3ORD == 4 && (w == 2 || w == 3));
        if (W3ORD != 1 && w3first) {
#pragma unroll
            for (int i = 0; i < 8; i++) ldw3(i);
        }
        SBAR();
        if (ODLY) { __builtin_amdgcn_s_sleep(ODLY); SBAR(); }
        u32x4 bf[8];
        auto ld = [&](int s) { return ld_sc1(rws, fo, OFF_O + (8 * kh + s) * PL); };
#pragma unroll
        for (int s = 0; s < 8; s++) bf[s] = ld(s);
        if (W3ORD != 1 && !w3first) {
            SBAR();
#pragma unroll
            for (int i = 0; i < 8; i++) ldw3(i);
        }
        if (CSMOVE & 2) { SBAR(); colsum1(nop_side); }
        verify<8>(bf, ld, dead);
        STAMP(7);
        const int r0 = 16 * (g4 >> 1) + 4 * (g4 & 1) + lq;
        unsigned wa[4];
#pragma unroll
        for (int nt = 0; nt < 4; nt++) wa[nt] = opq(sm + R0 + (256 * kh + r0) * 128 + ((nt ^ swz128(r0)) * 32) + lp * 8);
        f32x4 acc[4] = {};
        auto side3 = [&](int s) { if (W3ORD == 1) { ldw3(2 * s); ldw3(2 * s + 1); } else ldw3(8 + s); };
        if (GRING) gemm_r<8, 8 * 128, 4096, 4, GRING ? GRING : 1>(wa, bf, acc, side3); else gemm_p<8, 8 * 128, 4096, 4, GDP>(wa, bf, acc, side3);
        STAMP(26);
        xwrite(XS0, acc);
        bar();
        f32x4 v0, v1; xread(XS0, acc, v0, v1);
        STAMP(27);
        hres[0] = v0[0] + bflo(xres[0][0]); hres[1] = v0[1] + bfhi(xres[0][0]); hres[2] = v0[2] + bflo(xres[0][1]); hres[3] = v0[3] + bfhi(xres[0][1]);
        hres[4] = v1[0] + bflo(xres[1][0]); hres[5] = v1[1] + bfhi(xres[1][0]); hres[6] = v1[2] + bflo(xres[1][1]); hres[7] = v1[3] + bfhi(xres[1][1]);
        float mean, m2; piece_stats(hres, mean, m2);
        st_sc1(pack8(hres), rws, fo + (2 * j + kh) * PL, OFF_H);
        if (g4 == 0) st_sc1_64(u32x2{san(fbits(mean)), san(fbits(m2))}, rws, ((2 * j + kh) * 2048 + tokq) * 8, OFF_HS);
    }
    STAMP(8);
    {
        u32x4 bf[8]; u32x2 st[4];
        auto ld = [&](int s) { return ld_sc1(rws, fo, OFF_H + (8 * kh + s) * PL); };
        auto lds_ = [&](int i) { return ld_sc1_64(rws, ((4 * g4 + i) * 2048 + tokq) * 8, OFF_HS); };
        auto ldall = [&]() {
#pragma unroll
            for (int s = 0; s < 8; s++) bf[s] = ld(s);
#pragma unroll
            for (int i = 0; i < 4; i++) st[i] = lds_(i);
        };
        if (HBORD && w >= 4) {
#pragma unroll
            for (int s = 0; s < 8; s++) dma_sq(w2, 64 * w, R0 + w * 8192, s);
            __builtin_amdgcn_s_sleep(HBD); ldall(); SBAR();
        }
        if (CSMOVE & 2) {
#pragma unroll
            for (int s = 0; s < 8; s++) dma_sq(w2, 64 * w, R0 + w * 8192, s);
            if (HDLY) __builtin_amdgcn_s_sleep(HDLY);
        } else {
            colsum1([&](int s) { if (!HBORD || w < 4) dma_sq(w2, 64 * w, R0 + w * 8192, s); });
            if (HDLY) __builtin_amdgcn_s_sleep(HDLY);
        }
        STAMP(29);
        if (!HBORD || w < 4) ldall();
        verify<8>(bf, ld, dead);
        verify_st(st, lds_, dead);
        STAMP(9);
        float mu, var; combine_stats(st, mu, var);
        f32x4 acc[4] = {};
        gemm64(R1, bf, acc, [&](int s) { ldw3b(16 + s); });
        STAMP(30);
        const float r = __builtin_amdgcn_rsqf(var + 1e-5f);
        const float r2 = r * __builtin_amdgcn_rsqf(var * r * r + 1e-5f);
        if (!CSSH) {
#pragma unroll
            for (int nt = 0; nt < 4; nt++) acc[nt] -= mu * cs1[nt];
        }
        xwrite(XS1, acc);
        bar();
        f32x4 v0, v1; xread(XS1, acc, v0, v1);
        if (CSSH) {
            v0 -= mu * ldsrf4(sm + XS0 + ((2 * kh) ^ 4) * 2048 + g4 * 16);
            v1 -= mu * ldsrf4(sm + XS0 + ((2 * kh + 1) ^ 4) * 2048 + g4 * 16);
        }
        STAMP(31);
        float y[8];
#pragma unroll
        for (int i = 0; i < 4; i++) { y[i] = gelu(v0[i] * r2); y[4 + i] = gelu(v1[i] * r2); }
#pragma unroll
        for (int i = 0; i < 8; i++) xres1[i] = (hres[i] - mu) * r;
        st_sc1(pack8(y), rws, fo + (2 * j + kh) * PL, OFF_HH);
        STAMP(32);
        bar();
        if (HHORD == 0 || (HHORD == 2 && w < 4)) {
#pragma unroll
            for (int i = 24; i < 32; i++) ldw3(i);
        }
        SBAR();
    }
    STAMP(10);
    {
        const int dlt = kh ? 2 : 1;
        const int ta = tokq - dlt, tb = tokq + dlt;
        const unsigned va = ta >= 0 ? (g4 * 2048 + ta) * 16 : 0x80000000u;
        const unsigned vb = tb < 2048 ? (g4 * 2048 + tb) * 16 : 0x80000000u;
        if (HHDLY) { __builtin_amdgcn_s_sleep(HHDLY); SBAR(); }
        u32x4 bf[8];
        auto ld = [&](int s) { return ld_sc1(rws, s < 4 ? va : vb, OFF_HH + (8 * kh + s) * PL); };
#pragma unroll
        for (int s = 0; s < 8; s++) bf[s] = ld(s);
        if (HHORD == 1 || (HHORD == 2 && w >= 4)) {
            SBAR();
#pragma unroll
            for (int i = 24; i < 32; i++) ldw3(i);
        }
        if (CSMOVE & 1) { SBAR(); colsum3(0, 12); SBAR(); }
        verify<8>(bf, ld, dead);
        if (CSMOVE & 1) colsum3(12, 16);
        STAMP(11);
        f32x4 acc[4] = {};
        gemm64(R0, bf, acc, nop_side);
        STAMP(33);
        xwrite(XS0, acc);
        bar();
        f32x4 v0, v1; xread(XS0, acc, v0, v1);
#pragma unroll
        for (int i = 0; i < 4; i++) { xres1[i] += v0[i]; xres1[4 + i] += v1[i]; }
        STAMP(34);
        float mean, m2; piece_stats(xres1, mean, m2);
        st_sc1(pack8(xres1), rws, fo + (2 * j + kh) * PL, OFF_X1);
        if (g4 == 0) st_sc1_64(u32x2{san(fbits(mean)), san(fbits(m2))}, rws, ((2 * j + kh) * 2048 + tokq) * 8, OFF_XS);
        STAMP(35);
    }
    {
        constexpr unsigned XI = R0;
        constexpr unsigned STL = R2;
        float resid[8];
#pragma unroll
        for (int i = 0; i < 8; i++) resid[i] = hres[i] + xres1[i];
        const unsigned w4o = j * WT_J + w * 1024 + lane * 16;
        if (!(CSMOVE & 1)) colsum3(0, 16);
        if (XDLY) __builtin_amdgcn_s_sleep(XDLY);
        STAMP(36);
        if (SBARS & 2) bar();
        auto dmx = [&](int i) { dma16s(ws + OFF_X1 + (size_t)((8 * w + i) * 2048 + T0) * 16, lane * 16, sm + XI + (8 * w + i) * 1024); };
#pragma unroll
        for (int i = 0; i < 8; i++) dmx(i);
        u32x2 st[4];
        auto lds_ = [&](int i) { return ld_sc1_64(rws, ((4 * g4 + i) * 2048 + tokq) * 8, OFF_XS); };
#pragma unroll
        for (int i = 0; i < 4; i++) st[i] = lds_(i);
        STAMP(12);
        verify_st(st, lds_, dead);
        {
            float mu, var; combine_stats(st, mu, var);
            if (g4 == 0) *(LDSP f32x2*)(sm + STL + (16 * tq + t16) * 8) = f32x2{mu, __builtin_amdgcn_rsqf(var + 1e-5f)};
        }
        STAMP(37);
        if (XSEL) {
            auto piece = [&](int i) { return max4(*(volatile LDSP u32x4*)(sm + XI + (8 * w + i) * 1024 + lane * 16)); };
            waitvm<0>();
            unsigned m = 0;
#pragma unroll
            for (int i = 0; i < 8; i++) m = umax(m, piece(i));
            if (anyempty(m) && !dead) {
                int it = 0;
#pragma unroll 1
                for (; it < SPIN_MAX; it++) {
                    bool pend = false;
#pragma unroll
                    for (int i = 0; i < 8; i++) if (anyempty(piece(i))) { dmx(i); pend = true; }
                    if (!pend) break;
                    waitvm<0>();
                    if (VSLEEP) __builtin_amdgcn_s_sleep(VSLEEP);
                }
                if (it == SPIN_MAX) dead = 1;
            }
        } else
        for (int it = 0; it < SPIN_MAX && !dead; it++) {
            waitvm<0>();
            unsigned m = 0;
#pragma unroll
            for (int i = 0; i < 8; i++) m = umax(m, max4(*(volatile LDSP u32x4*)(sm + XI + (8 * w + i) * 1024 + lane * 16)));
            if (!anyempty(m)) break;
            if (VSLEEP) __builtin_amdgcn_s_sleep(VSLEEP);
#pragma unroll
            for (int i = 0; i < 8; i++) dmx(i);
            if (it == SPIN_MAX - 1) dead = 1;
        }
        bar();
        STAMP(13);
        f32x2 stt[4];
#pragma unroll
        for (int t = 0; t < 4; t++) stt[t] = *(LDSP f32x2*)(sm + STL + (16 * t + t16) * 8);
        const unsigned xa = opq(sm + XI + g4 * 1024 + t16 * 16);
        const unsigned go = w < 7 ? (g4 * 2048 + T0 + t16) * 16 + (7 * j + w) * PL : 0x80000000u;
        u32x4 w4f[28];
        auto half = [&](int h, f32x4 (&acc)[2][2], auto side) {
            constexpr int DP = 2;
            u32x4 xf[DP + 1][2];
#pragma unroll
            for (int d = 0; d < DP; d++) { xf[d][0] = ldsr128(xa + d * 4096 + h * 512); xf[d][1] = ldsr128(xa + d * 4096 + h * 512 + 256); }
#pragma unroll
            for (int s = 0; s < 16; s++) {
                SBAR();
                if (s + DP < 16) { xf[(s + DP) % (DP + 1)][0] = ldsr128(xa + (s + DP) * 4096 + h * 512); xf[(s + DP) % (DP + 1)][1] = ldsr128(xa + (s + DP) * 4096 + h * 512 + 256); }
                SBAR();
#pragma unroll
                for (int t = 0; t < 2; t++)
#pragma unroll
                    for (int c = 0; c < 2; c++) acc[t][c] = mfma16(asbf(w3f[2 * s + c]), asbf(xf[s % (DP + 1)][t]), acc[t][c]);
                SBAR();
                side(s);
            }
            SBAR();
        };
        f32x2 gcc = {-0.10294324f, -2.3022082f};
        asm volatile("" : "+v"(gcc));
        auto act = [&](const f32x4 (&acc)[2][2], int h, int t) {
            const float mu = stt[2 * h + t][0], rs = stt[2 * h + t][1];
            u32x4 o;
#pragma unroll
            for (int e = 0; e < 4; e++) {
                const int c = e >> 1, r = (e & 1) * 2;
                const f32x2 a = {acc[t][c][r], acc[t][c][r + 1]}, cv = {cs3[c][r], cs3[c][r + 1]};
                const f32x2 xv = (a - f32x2{mu, mu} * cv) * f32x2{rs, rs};
                const f32x2 y = GEL2 ? gelu2(xv, gcc) : f32x2{gelu(xv[0]), gelu(xv[1])};
                o[e] = san(pk2(y[0], y[1]));
            }
            return o;
        };
        auto ldw4 = [&](int i) { w4f[i] = ld_aux<W4AUX>(rws, w4o, OFF_W4T + i * 8192); };
        f32x4 aA[2][2] = {}, aB[2][2] = {};
        half(0, aA, [&](int s) {
            if (s < 10) {
                if (W4AUX) dma16s(ws + OFF_W4T + j * WT_J + w * 1024 + s * 8192, lane * 16, s < 8 ? sm + R1 + w * 8192 + s * 1024 : sm + XS1 + w * 2048 + (s - 8) * 1024);
                else dma16(ws + OFF_W4T + j * WT_J + w * 1024 + s * 8192, lane * 16, s < 8 ? sm + R1 + w * 8192 + s * 1024 : sm + XS1 + w * 2048 + (s - 8) * 1024);
            }
        });
        STAMP(39);
#pragma unroll
        for (int t = 0; t < 2; t++) st_sc1(act(aA, 0, t), rws, go + t * 256, OFF_G);
        STAMP(40);
        half(1, aB, [&](int s) {
            if (s < 2) { ldw4(10 + 2 * s); ldw4(11 + 2 * s); } else ldw4(12 + s);
        });
        STAMP(41);
        const unsigned gi = (g4 * 2048 + T0 + t16) * 16 + 7 * w * PL;
        auto ldg = [&](int n, int t) { return ld_sc1(rws, gi + (2 * (n / 7) + t) * 256, OFF_G + (n % 7) * PL); };
        constexpr int GD = GDEPTH;
        u32x4 g[GD + 1][2];
        if (EARLYG) {
#pragma unroll
            for (int d = 0; d < GD; d++) { g[d][0] = ldg(d, 0); g[d][1] = ldg(d, 1); }
            SBAR();
        }
#pragma unroll
        for (int t = 0; t < 2; t++) st_sc1(act(aB, 1, t), rws, go + (2 + t) * 256, OFF_G);
        STAMP(14);
        if (EARLYG) waitvm<22 + 2 * GD>(); else waitvm<22>();
        if (BARPOS == 0) bar();
        if (!EARLYG) {
            if (GDLY) { __builtin_amdgcn_s_sleep(GDLY); SBAR(); }
#pragma unroll
            for (int d = 0; d < GD; d++) { g[d][0] = ldg(d, 0); g[d][1] = ldg(d, 1); }
        }
#pragma unroll
        for (int i = 0; i < 10; i++) w4f[i] = ldsr128(i < 8 ? sm + R1 + w * 8192 + i * 1024 + lane * 16 : sm + XS1 + w * 2048 + (i - 8) * 1024 + lane * 16);
        f32x4 acc[2][4] = {};
#pragma unroll
        for (int n = 0; n < 14; n++) {
            const int ks = n % 7, sl = n % (GD + 1);
            SBAR();
            if (G2DLY && n + GD == 7) { __builtin_amdgcn_s_sleep(G2DLY); SBAR(); }
            if (n + GD < 14) { g[(n + GD) % (GD + 1)][0] = ldg(n + GD, 0); g[(n + GD) % (GD + 1)][1] = ldg(n + GD, 1); }
            SBAR();
            if (anyempty(umax(max4(g[sl][0]), max4(g[sl][1]))) && !dead) {
                int it = 0;
                for (; it < SPIN_MAX; it++) {
                    if (VSLEEP) __builtin_amdgcn_s_sleep(VSLEEP);
                    asm volatile("" ::: "memory");
#pragma unroll
                    for (int k = 0; k <= GD; k++) {
                        if (n + k < 14) {
#pragma unroll
                            for (int t = 0; t < 2; t++)
                                if (anyempty(max4(g[(n + k) % (GD + 1)][t]))) g[(n + k) % (GD + 1)][t] = ldg(n + k, t);
                        }
                    }
                    if (!anyempty(umax(max4(g[sl][0]), max4(g[sl][1])))) break;
                }
                if (it == SPIN_MAX) dead = 1;
            }
            SBAR();
#pragma unroll
            for (int t = 0; t < 2; t++)
#pragma unroll
                for (int c = 0; c < 4; c++) acc[t][c] = mfma16(asbf(w4f[4 * ks + c]), asbf(g[sl][t]), acc[t][c]);
            if (ks == 6) {
                SBAR();
                if (BARPOS == 1 && n == 13) bar();
                if (SCB) {
#pragma unroll
                    for (int t = 0; t < 2; t++)
#pragma unroll
                        for (int c = 0; c < 2; c++) {
                            const f32x4 a = acc[t][2 * c], bq = acc[t][2 * c + 1];
                            ldsw128(sm + (n == 6 ? R1 : R0) + w * 8192 + (2 * t + c) * 1024 + lane * 16, u32x4{pk2(a[0], a[1]), pk2(a[2], a[3]), pk2(bq[0], bq[1]), pk2(bq[2], bq[3])});
                            acc[t][2 * c] = f32x4{}; acc[t][2 * c + 1] = f32x4{};
                        }
                } else {
#pragma unroll
                for (int t = 0; t < 2; t++)
#pragma unroll
                    for (int c = 0; c < 4; c++) { ldswf4(sm + (n == 6 ? R1 : R0) + w * 8192 + (4 * t + c) * 1024 + lane * 16, acc[t][c]); acc[t][c] = f32x4{}; }
                }
                if (n == 6) STAMP(48);
            }
        }
        STAMP(15);
        bar();
        f32x4 v0 = {}, v1 = {};
        if (SCB) {
            const unsigned pa = sm + (tq < 2 ? R1 : R0) + ((tq & 1) * 2 + kh) * 1024 + lane * 16;
#pragma unroll
            for (int i = 0; i < 8; i++) {
                const u32x4 q = ldsr128(pa + i * 8192);
                v0 += f32x4{bflo(q[0]), bfhi(q[0]), bflo(q[1]), bfhi(q[1])};
                v1 += f32x4{bflo(q[2]), bfhi(q[2]), bflo(q[3]), bfhi(q[3])};
            }
        } else {
            const unsigned pa = sm + (tq < 2 ? R1 : R0) + ((tq & 1) * 4 + 2 * kh) * 1024 + lane * 16;
#pragma unroll
            for (int i = 0; i < 8; i++) { v0 += ldsrf4(pa + i * 8192); v1 += ldsrf4(pa + i * 8192 + 1024); }
        }
        u32x2 o0 = {pk2(resid[0] + v0[0], resid[1] + v0[1]), pk2(resid[2] + v0[2], resid[3] + v0[3])};
        u32x2 o1 = {pk2(resid[4] + v1[0], resid[5] + v1[1]), pk2(resid[6] + v1[2], resid[7] + v1[3])};
        if (OAUX == 0) {
            const unsigned xe = opq(xo);
            *(GASP u32x2*)(out + xe) = o0;
            *(GASP u32x2*)(out + xe + 32) = o1;
        } else {
            const rsrc_t ro = mkrsrc(out, 2048 * 1024);
            __builtin_amdgcn_raw_buffer_store_b64(o0, ro, tokq * 1024 + (64 * j + 32 * kh + 4 * g4) * 2, 0, OAUX);
            __builtin_amdgcn_raw_buffer_store_b64(o1, ro, tokq * 1024 + (64 * j + 32 * kh + 16 + 4 * g4) * 2, 0, OAUX);
        }
    }
    }
    STAMP(16); CSTAMP(63);
    asm volatile(".Lfbk_code_end:");
    STAMP_FLUSH();
}

extern "C" void kernel_launch(void* const* d_in, const int* in_sizes, int n_in, void* d_out, int out_size, void* d_ws, size_t ws_size,
                              hipStream_t stream) {
    (void)in_sizes; (void)n_in; (void)out_size; (void)ws_size;
    hipFuncSetAttribute((const void*)fused_block_kernel, hipFuncAttributeMaxDynamicSharedMemorySize, LDS_BYTES);
    fused_block_kernel<<<256, 512, LDS_BYTES, stream>>>((const char*)d_in[0], (const char*)d_in[1], (const char*)d_in[2], (const char*)d_in[3],
                                                        (const char*)d_in[4], (const char*)d_in[6], (const char*)d_in[7], (const char*)d_in[8],
                                                        (const char*)d_in[9], (char*)d_out, (char*)d_ws);
}
```
